```python
import torch

def _softmax(x, axis=-1):
    return torch.softmax(x, dim=axis)
def _gelu(x):
    return torch.nn.functional.gelu(x, approximate="tanh")
def _silu(x):
    return torch.nn.functional.silu(x)
def _sigmoid(x):
    return torch.sigmoid(x)
def _layernorm(x, axis=-1, eps=1e-5):
    m = x.mean(dim=axis, keepdim=True)
    v = x.var(dim=axis, keepdim=True, unbiased=False)
    return (x - m) / torch.sqrt(v + eps)
def _scatter_add(tgt, idx, src, axis=0):
    return tgt.clone().index_add_(axis, idx, src)
def _segment_sum(src, seg, n):
    out = torch.zeros((n,) + src.shape[1:], dtype=src.dtype)
    return out.index_add_(0, seg, src)
def _pad(x, pw):
    flat = tuple(v for pair in reversed(pw) for v in pair)
    return torch.nn.functional.pad(x, flat)

kernel_name = 'gather_scatter__edge_featured_mpnn_egc__graph'

def setup_inputs():

    _w = (lambda a: (lambda t: t.to(torch.bfloat16) if t.is_floating_point() else t)(torch.as_tensor(a)))
    N, deg, D, De = 10000, 16, 256, max(4, 1)
    E = N * deg
    return {
        "x": _w((lambda *s: torch.randn(*s, dtype=torch.bfloat16) * 0.1)(N, D)),
        "e_attr": _w((lambda *s: torch.randn(*s, dtype=torch.bfloat16) * 0.1)(E, De)),
        "src": _w((lambda lo, hi, *s: torch.randint(lo, hi, s))(0, N, E).to(torch.int64)),
        "dst": _w((lambda lo, hi, *s: torch.randint(lo, hi, s))(0, N, E).to(torch.int64)),
        "u_w": _w((lambda *s: torch.randn(*s, dtype=torch.bfloat16) * 0.05)(D, D)),
        "v_w": _w((lambda *s: torch.randn(*s, dtype=torch.bfloat16) * 0.05)(D, D)),
        "g_w": _w((lambda *s: torch.randn(*s, dtype=torch.bfloat16) * 0.1)(2 * D + De, D)),
    }

def reference(x, e_attr, src, dst, u_w, v_w, g_w):
    N, D = x.shape
    g_in = torch.concatenate([x[dst], x[src], e_attr], axis=-1)
    eta = _sigmoid(g_in @ g_w)
    hv = x @ v_w
    msg = eta * hv[src]
    agg = _segment_sum(msg, dst, N)
    return _silu(x @ u_w + agg)
```

```cpp
#include <hip/hip_runtime.h>
#include <hip/hip_bf16.h>
#include <stdint.h>

typedef unsigned int u32;
typedef unsigned long long u64;
typedef u32 u32x2 __attribute__((ext_vector_type(2)));
typedef u32 u32x3 __attribute__((ext_vector_type(3)));
typedef u32 u32x4 __attribute__((ext_vector_type(4)));
typedef float f32x2 __attribute__((ext_vector_type(2)));
typedef float f32x4 __attribute__((ext_vector_type(4)));
typedef __bf16 bf16x8 __attribute__((ext_vector_type(8)));

#define DEV __device__ __forceinline__

constexpr int D = 256;
constexpr int NB = 64;
constexpr int NPROD = 32;
constexpr int CAP = 128;
constexpr int TROWS = 160;
constexpr int MAXSTEPS = 896;
constexpr float NLOG2E = -1.4426950408889634f;
#define ROWPART 1

DEV u32 f2u(float f) { return __builtin_bit_cast(u32, f); }
DEV float u2f(u32 u) { return __builtin_bit_cast(float, u); }
DEV u32 bf16_bits(float f) { return (u32)__builtin_bit_cast(unsigned short, (__bf16)f); }
DEV u32 comp_word(float v, u32 lo) {
  u32 b = f2u(v);
  int t = (int)(b & 0x7fffffffu) + 0x8000 - (int)lo;
  t = t < 0 ? 0 : t;
  return ((u32)t & 0xffff0000u) | (b & 0x80000000u) | lo;
}

struct Params {
  const u32* x;
  const u32x2* ea;
  const int* src;
  const int* dst;
  const u32* uw;
  const u32* vw;
  const u32* gw;
  u32* T;
  u32* XA;
  u32* XU;
  u32* cells;
  u32* cnts;
  unsigned short* out;
  int N, E, BS, EPP;
};

__global__ __launch_bounds__(512, 4) void k1(Params p) {
  __shared__ __attribute__((aligned(16))) u32 lds[TROWS * 128];
  const int tid = threadIdx.x;
  if (blockIdx.x < NPROD) {
    const int pr = blockIdx.x;
    if (tid < NB) lds[tid] = 0;
    __syncthreads();
    const int e0 = pr * p.EPP, e1 = min(e0 + p.EPP, p.E);
    for (int eb = e0; eb < e1; eb += 512 * 5) {
      int dd[5], ss[5];
      u32x2 aa[5];
#pragma unroll
      for (int i = 0; i < 5; i++) {
        int e = eb + i * 512 + tid;
        bool ok = e < e1;
        int ec = ok ? e : e0;
        dd[i] = ok ? p.dst[ec] : -1;
        ss[i] = p.src[ec];
        aa[i] = p.ea[ec];
      }
#pragma unroll
      for (int i = 0; i < 5; i++) {
        if (dd[i] >= 0) {
          int b = dd[i] / p.BS;
          int dl = dd[i] - b * p.BS;
          u32 r = atomicAdd(&lds[b], 1u);
          if (r < CAP) {
            u32x3 rec = {(u32)ss[i] << 8 | (u32)dl, aa[i].x, aa[i].y};
            *(u32x3*)(p.cells + ((size_t)(b * NPROD + pr) * CAP + r) * 3) = rec;
          }
        }
      }
    }
    __syncthreads();
    if (tid < NB) p.cnts[tid * NPROD + pr] = lds[tid];
    return;
  }
  const int b = blockIdx.x - NPROD;
  const int s = (b >> 3) & 3, beta = (b & 7) * 8 + (b >> 5);
  const int n0 = beta * p.BS;
  const int nrows = min(p.BS, p.N - n0);
  if (nrows <= 0) return;
  {
    u32x4 v[10];
#pragma unroll
    for (int i = 0; i < 10; i++) {
      int c = i * 512 + tid, row = c >> 5, ch = c & 31;
      int rr = min(row, nrows - 1);
      v[i] = *(const u32x4*)(p.x + (size_t)(n0 + rr) * 128 + ch * 4);
    }
#pragma unroll
    for (int i = 0; i < 10; i++) {
      int c = i * 512 + tid, row = c >> 5, ch = c & 31;
      *(u32x4*)(lds + row * 128 + ((ch ^ (row & 15)) << 2)) = v[i];
    }
  }
  const int lane = tid & 63, w = __builtin_amdgcn_readfirstlane(tid >> 6);
  const int cg = w & 3, rh = w >> 2;
  const int g = lane >> 4, i16 = lane & 15;
  const bool isT = cg < 2;
  const int half = cg & 1;
  const u32* w0 = isT ? p.gw + 256 * 128 : p.gw;
  const u32* w1 = isT ? p.vw : p.uw;
  const int wcol = s * 32 + half * 16 + i16;
  f32x4 acc[2][2][5];
#pragma unroll
  for (int m = 0; m < 2; m++)
#pragma unroll
    for (int t = 0; t < 2; t++)
#pragma unroll
      for (int r = 0; r < 5; r++) acc[m][t][r] = f32x4{0.f, 0.f, 0.f, 0.f};
  u32 wr[2][8];
  const auto r0 = __builtin_amdgcn_make_buffer_rsrc((void*)w0, 0, 256 * 512, 0x00020000);
  const auto r1 = __builtin_amdgcn_make_buffer_rsrc((void*)w1, 0, 256 * 512, 0x00020000);
  const u32 wo = (g * 8 * 128 + wcol) * 4;
#pragma unroll
  for (int j = 0; j < 8; j++) {
    wr[0][j] = __builtin_amdgcn_raw_buffer_load_b32(r0, wo, j * 512, 0);
    wr[1][j] = __builtin_amdgcn_raw_buffer_load_b32(r1, wo, j * 512, 0);
  }
  __syncthreads();
  const u32* xrow = lds + (rh * 80 + i16) * 128;
#pragma unroll 1
  for (int ks = 0; ks < 8; ks++) {
    u32x4 af[2][2];
#pragma unroll
    for (int m = 0; m < 2; m++)
#pragma unroll
      for (int j = 0; j < 4; j++) {
        af[m][0][j] = __builtin_amdgcn_perm(wr[m][2 * j + 1], wr[m][2 * j], 0x05040100u);
        af[m][1][j] = __builtin_amdgcn_perm(wr[m][2 * j + 1], wr[m][2 * j], 0x07060302u);
      }
#pragma unroll
    for (int j = 0; j < 8; j++) {
      wr[0][j] = __builtin_amdgcn_raw_buffer_load_b32(r0, wo, (ks + 1) * 16384 + j * 512, 0);
      wr[1][j] = __builtin_amdgcn_raw_buffer_load_b32(r1, wo, (ks + 1) * 16384 + j * 512, 0);
    }
    const u32* xp = xrow + (((ks * 4 + g) ^ i16) << 2);
#pragma unroll
    for (int r = 0; r < 5; r++) {
      u32x4 bf = *(const u32x4*)(xp + r * 2048);
#pragma unroll
      for (int m = 0; m < 2; m++)
#pragma unroll
        for (int t = 0; t < 2; t++)
          acc[m][t][r] = __builtin_amdgcn_mfma_f32_16x16x32_bf16(
              __builtin_bit_cast(bf16x8, af[m][t]), __builtin_bit_cast(bf16x8, bf), acc[m][t][r], 0, 0, 0);
    }
  }
#pragma unroll
  for (int r = 0; r < 5; r++) {
    int row = (rh * 5 + r) * 16 + i16;
    if (row < nrows) {
      size_t node = (size_t)s * p.N + n0 + row;
      if (isT) {
        u32 wd[8];
#pragma unroll
        for (int q = 0; q < 4; q++) {
          float xb0 = __builtin_amdgcn_fmed3f(acc[0][0][r][q] * NLOG2E, -1e38f, 1e38f);
          float xb1 = __builtin_amdgcn_fmed3f(acc[0][1][r][q] * NLOG2E, -1e38f, 1e38f);
          u32 h0 = bf16_bits(acc[1][0][r][q]);
          u32 a = comp_word(xb0, h0);
          u32 h2 = comp_word(acc[1][1][r][q], a >> 16) >> 16;
          wd[2 * q] = a;
          wd[2 * q + 1] = comp_word(xb1, h2);
        }
        u32* dstp = p.T + node * 64 + half * 32 + g * 8;
        *(u32x4*)dstp = u32x4{wd[0], wd[1], wd[2], wd[3]};
        *(u32x4*)(dstp + 4) = u32x4{wd[4], wd[5], wd[6], wd[7]};
      } else {
        u32x4 a, u;
#pragma unroll
        for (int q = 0; q < 4; q++) {
          a[q] = bf16_bits(acc[0][0][r][q] * NLOG2E) | bf16_bits(acc[0][1][r][q] * NLOG2E) << 16;
          u[q] = bf16_bits(acc[1][0][r][q]) | bf16_bits(acc[1][1][r][q]) << 16;
        }
        *(u32x4*)(p.XA + node * 32 + half * 16 + g * 4) = a;
        *(u32x4*)(p.XU + node * 32 + half * 16 + g * 4) = u;
      }
    }
  }
}

struct K2Lds {
  u32x4 arec[MAXSTEPS * 4 + 16];
  u32 off[MAXSTEPS * 4];
  u32 xa[TROWS * 32];
  u32 xu[TROWS * 32];
  u32 cnt[192];
  u32 info[192];
  int order[192];
  u32 sdeg[192];
  u32 hist[64];
  u32 qs[64];
  u32x4 winfo[16];
  u32 flag[4];
};
constexpr u32 OOB = 0x7f000000u;

DEV float silu_f(float y) {
  float t = __builtin_amdgcn_exp2f(y * NLOG2E);
  return y * __builtin_amdgcn_rcpf(1.0f + t);
}

__global__ __launch_bounds__(1024) void k2(Params p) {
  __shared__ K2Lds L;
  const int tid = threadIdx.x, lane = tid & 63;
  const int w = __builtin_amdgcn_readfirstlane(tid >> 6);
  const int b = blockIdx.x, xcd = b & 7, s = xcd & 3, beta = (xcd >> 2) * 32 + (b >> 3);
  const int n0 = beta * p.BS;
  const int nrows = min(p.BS, p.N - n0);
  if (nrows <= 0) return;

  u32x3 rec[4];
  const u32* cb = p.cells + (size_t)beta * NPROD * CAP * 3;
#pragma unroll
  for (int i = 0; i < 4; i++) rec[i] = *(const u32x3*)(cb + (size_t)(i * 1024 + tid) * 3);
  u32 cc[4];
#pragma unroll
  for (int i = 0; i < 4; i++) cc[i] = p.cnts[beta * NPROD + ((i * 1024 + tid) >> 7)];
  u32x4 au[3];
  {
    const u32x4* xa = (const u32x4*)(p.XA + ((size_t)s * p.N + n0) * 32);
    const u32x4* xu = (const u32x4*)(p.XU + ((size_t)s * p.N + n0) * 32);
    int lim = nrows * 8 - 1;
    au[0] = xa[min(tid, lim)];
    au[1] = xu[min(tid, lim)];
    int t2 = 1024 + (tid & 255);
    au[2] = (tid & 256) ? xu[min(t2, lim)] : xa[min(t2, lim)];
  }
  u32 cb0, cb1;
  {
    const unsigned short* cw = (const unsigned short*)p.gw + 512 * 256 + s * 64 + (lane & 15) * 4 + (lane >> 4);
    float c0 = u2f((u32)cw[0] << 16), c1 = u2f((u32)cw[256] << 16), c2 = u2f((u32)cw[512] << 16),
          c3 = u2f((u32)cw[768] << 16);
    cb0 = bf16_bits(c0 * NLOG2E) | bf16_bits(c1 * NLOG2E) << 16;
    cb1 = bf16_bits(c2 * NLOG2E) | bf16_bits(c3 * NLOG2E) << 16;
  }
  for (int i = tid; i < MAXSTEPS * 4 + 16; i += 1024) L.arec[i] = u32x4{0, 0, 0, 0};
  if (tid < MAXSTEPS) *(u32x4*)&L.off[tid * 4] = u32x4{OOB, OOB, OOB, OOB};
  if (tid < 192) { L.cnt[tid] = 0; L.order[tid] = -1; L.sdeg[tid] = 0; }
  if (tid < 64) L.hist[tid] = 0;
  if (tid < 4) L.flag[tid] = 0;
  __syncthreads();
  u32 rank[4];
  bool bad = false;
#pragma unroll
  for (int i = 0; i < 4; i++) {
    bool ok = (u32)(tid & (CAP - 1)) < cc[i];
    bad |= cc[i] > CAP;
    rank[i] = 0xffffffffu;
    if (ok) rank[i] = atomicAdd(&L.cnt[rec[i].x & 255], 1u);
  }
  if (bad) L.flag[0] = 1;
  ((u32x4*)L.xa)[tid] = au[0];
  ((u32x4*)L.xu)[tid] = au[1];
  if (tid < 512) ((u32x4*)((tid & 256) ? L.xu : L.xa))[1024 + (tid & 255)] = au[2];
  __syncthreads();
  if (w == 0) {
    u32 deg[3], bin[3], rk[3];
#pragma unroll
    for (int k = 0; k < 3; k++) {
      int n = lane + 64 * k;
      deg[k] = L.cnt[n];
      bin[k] = 63 - min(deg[k], 63u);
      rk[k] = 0;
      if (n < nrows) rk[k] = atomicAdd(&L.hist[bin[k]], 1u);
    }
    __builtin_amdgcn_fence(__ATOMIC_SEQ_CST, "workgroup");
    u32 h = L.hist[lane], inc = h;
#pragma unroll
    for (int o = 1; o < 64; o <<= 1) {
      u32 v = __shfl_up(inc, o);
      if (lane >= o) inc += v;
    }
    u32 start = inc - h;
    u32 pos[3];
#pragma unroll
    for (int k = 0; k < 3; k++) {
      int n = lane + 64 * k;
      pos[k] = __shfl(start, bin[k]) + rk[k];
      if (n < nrows) { L.order[pos[k]] = n; L.sdeg[pos[k]] = deg[k]; }
    }
    __builtin_amdgcn_fence(__ATOMIC_SEQ_CST, "workgroup");
    u32x4 d4 = *(u32x4*)&L.sdeg[min(lane, 47) * 4];
    u32 len = max(max(d4.x, d4.y), max(d4.z, d4.w));
    len = min(max(len, 1u), 100000u);
    if (lane * 4 >= nrows) len = 0;
    int q0 = lane & 15, q1 = 31 - q0, q2 = 47 - q0;
    u32 l0 = __shfl(len, q0), l1 = __shfl(len, q1), l2 = __shfl(len, q2);
    u32 tot = ((l0 + l1 + l2 + 3) & ~3u) + 8, winc = tot;
#pragma unroll
    for (int o = 1; o < 16; o <<= 1) {
      u32 v = __shfl_up(winc, o, 16);
      if (q0 >= o) winc += v;
    }
    u32 wb = winc - tot;
    if (lane < 16) {
      L.qs[q0] = wb; L.qs[q1] = wb + l0; L.qs[q2] = wb + l0 + l1;
      L.winfo[q0] = u32x4{wb, l0, l1, l2};
      if (lane == 15 && winc > MAXSTEPS) L.flag[0] = 1;
    }
    __builtin_amdgcn_fence(__ATOMIC_SEQ_CST, "workgroup");
#pragma unroll
    for (int k = 0; k < 3; k++) {
      int n = lane + 64 * k;
      if (n < nrows) L.info[n] = L.qs[pos[k] >> 2] << 2 | (pos[k] & 3);
    }
  }
  __syncthreads();
  const auto trs = __builtin_amdgcn_make_buffer_rsrc((void*)(p.T + (size_t)s * p.N * 64), 0, p.N * 256, 0x00020000);
  const int q = lane >> 4, j = lane & 15;
  bool generic = L.flag[0] != 0;
  if (!generic) {
#pragma unroll
    for (int i = 0; i < 4; i++) {
      if (rank[i] != 0xffffffffu) {
        u32 inf = L.info[rec[i].x & 255];
        u32 t = (inf >> 2) + rank[i], qq = inf & 3;
        L.off[(t >> 2) * 16 + qq * 4 + (t & 3)] = rec[i].x & 0xffffff00u;
        u32 one = (qq & 1) ? 0x3f800000u : 0x00003f80u;
        L.arec[16 + t * 4 + qq] = u32x4{rec[i].y, rec[i].z, (qq & 2) ? 0u : one, (qq & 2) ? one : 0u};
      }
    }
    __syncthreads();
    u32x4 wi = L.winfo[w];
    const int wb = __builtin_amdgcn_readfirstlane(wi.x);
    const int l0 = __builtin_amdgcn_readfirstlane(wi.y), l1 = __builtin_amdgcn_readfirstlane(wi.z),
              l2 = __builtin_amdgcn_readfirstlane(wi.w);
    if (l0 > 0) {
      const bool av = (q == (j & 3));
      const u32x4* ap = av ? &L.arec[16 + wb * 4 + (j >> 2)] : &L.arec[0];
      const int ainc = av ? 16 : 0;
      const u32x4* op = (const u32x4*)&L.off[wb * 4 + q * 4];
      const u32 lo16 = j * 16;
      int quad = w, round = 0, rem = l0;
      u32x4 bop;
      bop.x = cb0; bop.y = cb1;
      int mynode;
      auto load_quad = [&](int qd) {
        u32x4 o4 = *(const u32x4*)&L.order[qd * 4];
        const unsigned short* xa = (const unsigned short*)L.xa + j * 4 + q;
        u32 a0 = xa[max((int)o4.x, 0) * 64], a1 = xa[max((int)o4.y, 0) * 64], a2 = xa[max((int)o4.z, 0) * 64],
            a3 = xa[max((int)o4.w, 0) * 64];
        bop.z = a0 | a1 << 16; bop.w = a2 | a3 << 16;
        mynode = L.order[qd * 4 + q];
      };
      load_quad(quad);
      f32x2 N0 = {0.f, 0.f}, N1 = {0.f, 0.f}, D0 = {1.f, 1.f}, D1 = {1.f, 1.f};
      u32x4 gq[8];
      {
        u32x4 o0 = op[0], o1 = op[4];
#pragma unroll
        for (int i = 0; i < 4; i++) gq[i] = __builtin_amdgcn_raw_buffer_load_b128(trs, o0[i] + lo16, 0, 0);
#pragma unroll
        for (int i = 0; i < 4; i++) gq[4 + i] = __builtin_amdgcn_raw_buffer_load_b128(trs, o1[i] + lo16, 0, 0);
      }
      op += 8;
      bool badn = false;
      for (;;) {
#pragma unroll
        for (int blk = 0; blk < 2; blk++) {
          u32x4 on = op[0];
          op += 4;
          u32x4 a4[4];
#pragma unroll
          for (int i = 0; i < 4; i++) a4[i] = ap[i * 4 * 0 + (av ? i * 4 : i)];
          ap += ainc;
#pragma unroll
          for (int i = 0; i < 4; i++) {
            u32x4 gv = gq[blk * 4 + i];
            f32x4 z = __builtin_amdgcn_mfma_f32_16x16x32_bf16(__builtin_bit_cast(bf16x8, a4[i]),
                                                              __builtin_bit_cast(bf16x8, bop),
                                                              __builtin_bit_cast(f32x4, gv), 0, 0, 0);
            u64 s0 = ((u64)gv.y << 32 | gv.x) << 16, s1 = ((u64)gv.w << 32 | gv.z) << 16;
            gq[blk * 4 + i] = __builtin_amdgcn_raw_buffer_load_b128(trs, on[i] + lo16, 0, 0);
            f32x2 h0 = __builtin_bit_cast(f32x2, s0), h1 = __builtin_bit_cast(f32x2, s1);
            f32x2 t0 = {__builtin_amdgcn_exp2f(z.x), __builtin_amdgcn_exp2f(z.y)};
            f32x2 t1 = {__builtin_amdgcn_exp2f(z.z), __builtin_amdgcn_exp2f(z.w)};
            f32x2 u0 = __builtin_elementwise_fma(h0, D0, N0), u1 = __builtin_elementwise_fma(h1, D1, N1);
            N0 = __builtin_elementwise_fma(N0, t0, u0);
            N1 = __builtin_elementwise_fma(N1, t1, u1);
            D0 = __builtin_elementwise_fma(D0, t0, D0);
            D1 = __builtin_elementwise_fma(D1, t1, D1);
            if (--rem == 0) {
              u32x2 xu = *(const u32x2*)&L.xu[max(mynode, 0) * 32 + j * 2];
              float y0 = N0.x * __builtin_amdgcn_rcpf(D0.x), y1 = N0.y * __builtin_amdgcn_rcpf(D0.y);
              float y2 = N1.x * __builtin_amdgcn_rcpf(D1.x), y3 = N1.y * __builtin_amdgcn_rcpf(D1.y);
              float dm = fmaxf(fmaxf(D0.x, D0.y), fmaxf(D1.x, D1.y));
              float chk = (D0.x + D0.y) + (D1.x + D1.y) + fabsf(N0.x) + fabsf(N0.y) + fabsf(N1.x) + fabsf(N1.y);
              badn |= !(dm < 1e36f) || !(chk < 3e38f);
              y0 += u2f(xu.x << 16); y1 += u2f(xu.x & 0xffff0000u);
              y2 += u2f(xu.y << 16); y3 += u2f(xu.y & 0xffff0000u);
              u32x2 o;
              o.x = bf16_bits(silu_f(y0)) | bf16_bits(silu_f(y1)) << 16;
              o.y = bf16_bits(silu_f(y2)) | bf16_bits(silu_f(y3)) << 16;
              if (mynode >= 0) *(u32x2*)(p.out + (size_t)(n0 + mynode) * D + s * 64 + j * 4) = o;
              round++;
              rem = round == 1 ? l1 : (round == 2 ? l2 : 0);
              if (rem == 0) goto done;
              quad = round == 1 ? 31 - w : 47 - w;
              load_quad(quad);
              N0 = N1 = f32x2{0.f, 0.f};
              D0 = D1 = f32x2{1.f, 1.f};
            }
          }
        }
      }
    done:
      if (__any(badn)) L.flag[1] = 1;
    }
    __syncthreads();
    generic = L.flag[1] != 0;
  }
  if (generic) {
    float* agg = (float*)L.arec;
    __syncthreads();
    for (int i = tid; i < TROWS * 64; i += 1024) agg[i] = 0.f;
    __syncthreads();
    const unsigned short* cw = (const unsigned short*)p.gw + 512 * 256 + s * 64 + lane;
    float c0 = u2f((u32)cw[0] << 16) * NLOG2E, c1 = u2f((u32)cw[256] << 16) * NLOG2E,
          c2 = u2f((u32)cw[512] << 16) * NLOG2E, c3 = u2f((u32)cw[768] << 16) * NLOG2E;
    const u32* Ts = p.T + (size_t)s * p.N * 64;
    for (int eb = w * 64; eb < p.E; eb += 1024) {
      int e = eb + lane;
      int dl = e < p.E ? p.dst[e] - n0 : -1;
      u64 m = __ballot(dl >= 0 && dl < nrows);
      while (m) {
        int k = __builtin_ctzll(m);
        m &= m - 1;
        int dk = __shfl(dl, k);
        int sk = p.src[eb + k];
        u32x2 a = p.ea[eb + k];
        u32 wd = Ts[(size_t)sk * 64 + lane];
        u32 wprev = Ts[(size_t)sk * 64 + (lane & ~1)];
        float hv = (lane & 1) ? u2f(wd << 16 | wprev >> 16) : u2f(wd << 16);
        float z = u2f(wd) + u2f((u32)((const unsigned short*)L.xa)[dk * 64 + lane] << 16);
        z += u2f(a.x << 16) * c0 + u2f(a.x & 0xffff0000u) * c1 + u2f(a.y << 16) * c2 + u2f(a.y & 0xffff0000u) * c3;
        atomicAdd(&agg[dk * 64 + lane], hv * __builtin_amdgcn_rcpf(1.0f + __builtin_amdgcn_exp2f(z)));
      }
    }
    __syncthreads();
    for (int i = tid; i < nrows * 64; i += 1024) {
      float y = agg[i] + u2f((u32)((const unsigned short*)L.xu)[i] << 16);
      p.out[(size_t)(n0 + (i >> 6)) * D + s * 64 + (i & 63)] = (unsigned short)bf16_bits(silu_f(y));
    }
  }
}

extern "C" void kernel_launch(void* const* d_in, const int* in_sizes, int n_in, void* d_out, int out_size,
                              void* d_ws, size_t ws_size, hipStream_t stream) {
  Params p;
  p.x = (const u32*)d_in[0];
  p.ea = (const u32x2*)d_in[1];
  p.src = (const int*)d_in[2];
  p.dst = (const int*)d_in[3];
  p.uw = (const u32*)d_in[4];
  p.vw = (const u32*)d_in[5];
  p.gw = (const u32*)d_in[6];
  p.N = in_sizes[0] / D;
  p.E = in_sizes[2];
  p.BS = (p.N + NB - 1) / NB;
  p.EPP = (p.E + NPROD - 1) / NPROD;
  char* ws = (char*)d_ws;
  size_t o = 0;
  p.T = (u32*)(ws + o); o += (size_t)p.N * 1024;
  p.XA = (u32*)(ws + o); o += (size_t)p.N * 512;
  p.XU = (u32*)(ws + o); o += (size_t)p.N * 512;
  p.cells = (u32*)(ws + o); o += (size_t)NB * NPROD * CAP * 12;
  p.cnts = (u32*)(ws + o);
  p.out = (unsigned short*)d_out;
  k1<<<NPROD + 256, 512, 0, stream>>>(p);
  k2<<<256, 1024, 0, stream>>>(p);
}
```

```cpp
#include <hip/hip_runtime.h>
#include <hip/hip_bf16.h>
#include <stdint.h>

typedef unsigned int u32;
typedef unsigned long long u64;
typedef unsigned short u16;
typedef u32 u32x2 __attribute__((ext_vector_type(2)));
typedef u32 u32x3 __attribute__((ext_vector_type(3)));
typedef u32 u32x4 __attribute__((ext_vector_type(4)));
typedef float f32x2 __attribute__((ext_vector_type(2)));
typedef float f32x4 __attribute__((ext_vector_type(4)));
typedef short s16x4 __attribute__((ext_vector_type(4)));
typedef __bf16 bf16x8 __attribute__((ext_vector_type(8)));
typedef __bf16 bf16x2 __attribute__((ext_vector_type(2)));

#define DEV __device__ __forceinline__
#define LAS __attribute__((address_space(3)))
#define GAS __attribute__((address_space(1)))
#define CBAR() asm volatile("" ::: "memory")

constexpr int D = 256;
constexpr int NB = 64;
constexpr int NWG = 256;
constexpr int EPPMAX = 640;
constexpr int RECSTRIDE = EPPMAX * 12;
constexpr int MAXSTEPS = 736;
constexpr int AUP = 264;
constexpr int XPAIR = 1056;
constexpr float NLOG2E = -1.4426950408889634f;
constexpr float TINY = 7.888609052210118e-31f;
constexpr u32 OOB = 0x7f000000u;
constexpr int SPINCAP = 1 << 20;
constexpr u32 RINC = 32u | 8u << 16;
constexpr int XFIRST = 6;
constexpr int XDEPTH = 8;
constexpr int WFIRST = 4;
constexpr int PFN = 2;
constexpr int CODELEN = 20480;

constexpr int L_CTRL = 0;
constexpr int L_CNT = 128;
constexpr int L_HIST = L_CNT + 3840;
constexpr int L_HIST1 = L_HIST + 256;
constexpr int L_QLEN = L_HIST1 + 256;
constexpr int L_QS = L_QLEN + 256;
constexpr int L_WINFO = L_QS + 256;
constexpr int L_ORDER = L_WINFO + 256;
constexpr int L_X = 5888;
constexpr int L_Y = L_X + 80 * XPAIR;
constexpr int L_EA = L_Y;
constexpr int L_IDX = L_EA + (MAXSTEPS * 4 + 64) * 8;
constexpr int L_XA = L_IDX + (MAXSTEPS + 144) * 8;
constexpr int L_XU = L_XA + 128;
constexpr int L_END = L_XA + 160 * AUP;
constexpr int L_STG = L_Y + 65536;
static_assert(L_END <= 163840 && L_STG + RECSTRIDE + 16 <= L_END && L_ORDER + 400 <= L_X, "lds");
enum { C_TAGLO = 0, C_TAGHI, C_H1, C_H2, C_C1, C_C2, C_TDONE, C_PDONE, C_P1, C_LAYOUT, C_LOCAL, C_TRDY, C_GEN,
       C_XT = 16   };

DEV u32 f2u(float f) { return __builtin_bit_cast(u32, f); }
DEV float u2f(u32 u) { return __builtin_bit_cast(float, u); }
DEV u32 pk_bf16(float a, float b) { return __builtin_bit_cast(u32, bf16x2{(__bf16)a, (__bf16)b}); }
DEV float silu_f(float y) { return y * __builtin_amdgcn_rcpf(1.0f + __builtin_amdgcn_exp2f(y * NLOG2E)); }
DEV int fresh_lane() {
  int ln;
  asm volatile("v_mbcnt_lo_u32_b32 %0, -1, 0\n\tv_mbcnt_hi_u32_b32 %0, -1, %0" : "=v"(ln));
  return ln;
}
DEV u32 scan_row16(u32 v) {
  v += __builtin_amdgcn_update_dpp(0u, v, 0x111, 0xf, 0xf, true);
  v += __builtin_amdgcn_update_dpp(0u, v, 0x112, 0xf, 0xf, true);
  v += __builtin_amdgcn_update_dpp(0u, v, 0x114, 0xf, 0xf, true);
  v += __builtin_amdgcn_update_dpp(0u, v, 0x118, 0xf, 0xf, true);
  return v;
}
DEV u32 scan64(u32 v) {
  v = scan_row16(v);
  v += __builtin_amdgcn_update_dpp(0u, v, 0x142, 0xa, 0xf, false);
  v += __builtin_amdgcn_update_dpp(0u, v, 0x143, 0xc, 0xf, false);
  return v;
}


struct Params {
  const u32* x;
  const u32* ea;
  const int* src;
  const int* dst;
  const u32* uw;
  const u32* vw;
  const u32* gw;
  u32* T;
  u32* recs;
  u64* gran;
  u64* tflag;
  u64* gen;
  u16* out;
  int N, E, BS, EPP;
  u32 magic, pad;
};

#define RSRC(ptr, stride, n) __builtin_amdgcn_make_buffer_rsrc((void*)(ptr), (stride), (n), 0x00020000)
#define LD_SC1(ptr) __hip_atomic_load((const GAS u64*)(ptr), __ATOMIC_RELAXED, __HIP_MEMORY_SCOPE_AGENT)
#define ST_SC1(ptr, v) __hip_atomic_store((GAS u64*)(ptr), (v), __ATOMIC_RELAXED, __HIP_MEMORY_SCOPE_AGENT)
#define LDS_ADD(w, v) __hip_atomic_fetch_add((LAS u32*)(w), (v), __ATOMIC_RELAXED, __HIP_MEMORY_SCOPE_WORKGROUP)
#define WAITGE(word, val) do { _Pragma("unroll 1") for (int it_ = 0; it_ < SPINCAP && ctrl[word] < (u32)(val); it_++) __builtin_amdgcn_s_sleep(1); CBAR(); } while (0)
#define SIGNAL(word) asm volatile("s_mov_b64 exec, 1\n\tds_add_u32 %0, %1 offset:%2\n\ts_mov_b64 exec, -1" :: "v"((u32)(size_t)S), "v"(1u), "i"(L_CTRL + (word) * 4) : "memory")
#define LGKM0() asm volatile("s_waitcnt lgkmcnt(0)" ::: "memory")
#define VMCNT(n) asm volatile("s_waitcnt vmcnt(" #n ")" ::: "memory")

template <class R>
DEV void dma16(u32 ldsaddr, u32 voff, R rs, u32 soff) {
  asm volatile("s_mov_b32 m0, %0\n\ts_nop 0\n\tbuffer_load_dwordx4 %1, %2, %3 offen lds"
               :: "s"(ldsaddr), "v"(voff), "s"(rs), "s"(soff) : "memory");
}

__global__ __launch_bounds__(1024, 4) void fused(Params p) {
  __shared__ __attribute__((aligned(16))) char smem[L_END];
  LAS char* const S = (LAS char*)smem;
  volatile LAS u32* const ctrl = (volatile LAS u32*)(S + L_CTRL);
  const int w = __builtin_amdgcn_readfirstlane(threadIdx.x >> 6);
  const int b = blockIdx.x, xcd = b & 7, s = xcd & 3, beta = (xcd >> 2) * 32 + (b >> 3);
  const int n0 = beta * p.BS;
  const int nrows = max(min(p.BS, p.N - n0), 0);
  const int rot = s;

#define WSETUP(ptr, m) const auto wrs = RSRC(ptr, 0, 256 * 512); \
    const u32 wv = (lane >> 3) * 512 + s * 128 + (((((lane & 7) >> 1) ^ (((lane >> 4) & 1) | ((m) << 1))) << 1) | (lane & 1)) * 16;
#define WI(m, pc) dma16((u32)(size_t)(S + L_Y) + (m) * 32768 + (pc) * 1024, wv, wrs, (pc) * 4096)
  {
  const int lane = fresh_lane(), q = lane >> 4, j = lane & 15, cu = b >> 3;
  if (w == 0) {
    __builtin_amdgcn_s_setprio(2);
    const auto xrs = RSRC(p.x, 0, p.N * 512);
    const u32 xbase = (u32)(size_t)(S + L_X), xv = (lane >> 5) * 512 + ((lane & 31) ^ (lane >> 5)) * 16;
    asm volatile("s_nop 4");
#pragma unroll 1
    for (int g = 0; g < 20; g++) {
      int gg = g + rot * 4;
      gg -= gg >= 20 ? 20 : 0;
#pragma unroll
      for (int k = 0; k < 4; k++) dma16(xbase + (g * 4 + k) * XPAIR, xv, xrs, n0 * 512 + (gg * 4 + k) * 1024);
      if (g == XFIRST - 1) { __builtin_amdgcn_s_barrier(); }
      if (g >= XFIRST - 1) {
        asm volatile("s_waitcnt vmcnt(%0)" :: "i"(4 * XDEPTH - 4) : "memory");
        const int done = g + 2 - XDEPTH;
        if (lane < (done >> 2)) ctrl[C_XT + lane] = 1;
      }
    }
    VMCNT(48); if (lane < 2) ctrl[C_XT + lane] = 1;
    VMCNT(32); ctrl[C_XT + 2] = 1;
    VMCNT(16); ctrl[C_XT + 3] = 1;
    VMCNT(0); ctrl[C_XT + 4] = 1;
    WAITGE(C_TDONE, 4);
    const u64 tg = *(volatile LAS u64*)&ctrl[C_TAGLO];
    const u64* tf = p.tflag + (s * NB + lane) * 8;
    const auto prs = RSRC(p.T + (size_t)s * p.N * 64, 0, p.N * 256);
    bool ok = false, par = false;
    int pk = 0;
#pragma unroll 1
    for (int it = 0; it < SPINCAP; it++) {
      u64 fv = tg;
      if (!ok) fv = LD_SC1(tf);
      u32 t[PFN];
#pragma unroll
      for (int u = 0; u < PFN; u++) {
        t[u] = 0;
        if (pk < 5 || (par && pk < 10)) {
          const int o = (pk < 5 ? pk : pk - 5) * 8192 + lane * 128;
          t[u] = __builtin_amdgcn_raw_buffer_load_b32(prs, o < p.BS * 256 ? o : OOB, (pk < 5 ? beta : beta ^ 32) * p.BS * 256, 16);
          pk++;
        }
      }
      ok = fv == tg;
      const u64 okm = __ballot(ok);
      if (okm == ~0ull) {
        CBAR();
        ctrl[C_TRDY] = 1;
        if (pk >= 10 || ctrl[C_LOCAL] >= 14) break;
      }
      par = (okm >> (beta ^ 32)) & 1;
#pragma unroll
      for (int u = 0; u < PFN; u++) asm volatile("" :: "v"(t[u]));
      __builtin_amdgcn_s_sleep(2);
    }
    CBAR();
    ctrl[C_TRDY] = 1;
  } else if (w == 1) {
    __builtin_amdgcn_s_setprio(3);
    {
      LAS u32x4* z = (LAS u32x4*)S;
#pragma unroll
      for (int k = 0; k < (L_X + 1023) / 1024; k++) {
        u32 v = k * 64 + lane < L_ORDER / 16 ? 0u : ~0u;
        if (k * 64 + lane < L_X / 16) z[k * 64 + lane] = u32x4{v, v, v, v};
      }
      LGKM0();
    }
    __builtin_amdgcn_s_barrier();
    WAITGE(C_C2, 4);
    {
      LAS u32x4* z = (LAS u32x4*)(S + L_EA);
#pragma unroll
      for (int k = 0; k < (L_IDX - L_EA + 1023) / 1024; k++) z[k * 64 + lane] = u32x4{0, 0, 0, 0};
      CBAR();
      z = (LAS u32x4*)(S + L_IDX);
#pragma unroll
      for (int k = 0; k < (L_XA - L_IDX + 1023) / 1024; k++)
        if (k * 64 + lane < (L_XA - L_IDX) / 16) z[k * 64 + lane] = u32x4{~0u, ~0u, ~0u, ~0u};
    }
    {
      WAITGE(C_P1, 6);
      u32 cw[3][6], deg[3], bin[3], r2[3];
#pragma unroll
      for (int k = 0; k < 3; k++)
#pragma unroll
        for (int i = 0; i < 6; i++) cw[k][i] = *(LAS u32*)(S + L_CNT + i * 640 + min(lane + 64 * k, 159) * 4);
#pragma unroll
      for (int k = 0; k < 3; k++) {
#pragma unroll
        for (int i = 1; i < 6; i++) cw[k][i] += cw[k][i - 1];
        deg[k] = cw[k][5] >> 19;
        bin[k] = 63 - min(deg[k], 63u);
        r2[k] = 1024;
        if (lane + 64 * k < nrows) r2[k] = LDS_ADD(S + L_HIST + bin[k] * 4, 1u);
      }
      CBAR();
      const u32 h = *(volatile LAS u32*)(S + L_HIST + lane * 4);
      const u32 st = scan64(h) - h;
      u32 pos[3];
#pragma unroll
      for (int k = 0; k < 3; k++) pos[k] = __builtin_amdgcn_ds_bpermute(bin[k] * 4, st);
      __builtin_amdgcn_sched_barrier(0);
#pragma unroll
      for (int k = 0; k < 3; k++) {
        pos[k] = min(pos[k] + r2[k], 192u);
        __hip_atomic_fetch_max((LAS u32*)(S + L_QLEN + (pos[k] >> 2) * 4), max(deg[k], 1u), __ATOMIC_RELAXED, __HIP_MEMORY_SCOPE_WORKGROUP);
      }
      CBAR();
      volatile LAS u32* ql = (volatile LAS u32*)(S + L_QLEN);
      volatile LAS u32* qs = (volatile LAS u32*)(S + L_QS);
      const u32 l0 = ql[j], l1 = ql[31 - j], l2 = ql[47 - j];
#pragma unroll
      for (int k = 0; k < 3; k++) *(volatile LAS u16*)(S + L_ORDER + pos[k] * 2) = (u16)(lane + 64 * k);
      const u32 tot = l0 + l1 + l2, inc = scan_row16(tot), wb = inc - tot;
      const u32 q0 = wb * RINC + (j << 22);
      qs[j] = q0;
      qs[31 - j] = q0 + l0 * RINC;
      qs[47 - j] = q0 + (l0 + l1) * RINC;
      u32 v0[3];
#pragma unroll
      for (int k = 0; k < 3; k++) v0[k] = qs[pos[k] >> 2];
      *(volatile LAS u32x4*)(S + L_WINFO + j * 16) = u32x4{wb, l0, l1, l2};
      if (inc > MAXSTEPS) ctrl[C_GEN] = 1;
#pragma unroll
      for (int k = 0; k < 3; k++) {
        const u32 x0 = v0[k] + (pos[k] & 3) * (RINC / 4);
        if (k < 2 || lane < 32) {
#pragma unroll
          for (int i = 0; i < 6; i++) *(LAS u32*)(S + L_CNT + i * 640 + (lane + 64 * k) * 4) = i ? x0 + cw[k][i - 1] : x0;
        }
      }
      CBAR();
      ctrl[C_LAYOUT] = 1;
      if (b == 0 && lane == 0) ST_SC1(p.gen, *(volatile LAS u64*)&ctrl[C_TAGLO]);
    }
  } else if (w >= 8) {
    const int cg = w & 3, au = (w >> 2) & 1;
    __builtin_amdgcn_s_setprio(1);
    {
      WSETUP(au ? p.vw : p.gw + 256 * 128, au);
      asm volatile("s_nop 4");
#pragma unroll
      for (int k = 0; k < 8; k++) {
        WI(au, cg * 8 + ((k + cu) & 7));
        if (k == WFIRST - 1) __builtin_amdgcn_s_barrier();
      }
    }
    const u32 ybase = (u32)(size_t)(S + L_Y);
#define GSETUP() const int ln = fresh_lane(), q = ln >> 4, j = ln & 15, qq = j >> 2, pp = j & 3, h = ln >> 5, n = ln & 31; \
    u32 wt = ybase + (q & 1) * 32768 + (h * 8 + qq) * 128 + ((cg ^ ((qq >> 1) | ((q & 1) << 1))) << 5) + pp * 8; \
    u32 xr = (u32)(size_t)(S + L_X) + (n >> 1) * XPAIR + (n & 1) * 512 + ((h ^ n) & 1) * 16; \
    u32 fl = (u32)(size_t)&ctrl[C_XT];
    u32 gi, gc;
    VMCNT(0);
    SIGNAL(C_H1);
    if (au) {
      {
        WSETUP((cg & 1) ? p.uw : p.gw, cg & 1);
        __builtin_amdgcn_s_setprio(3);
        WAITGE(C_C1, 4);
#pragma unroll
        for (int k = 0; k < 16; k++) WI(cg & 1, (cg >> 1) * 16 + ((k + cu) & 15));
      }
      __builtin_amdgcn_s_setprio(2);
      GSETUP();
      VMCNT(0);
      SIGNAL(C_H2);
      u32 no = rot * 32 * AUP, po = no;
      asm volatile(
        "v_mov_b32 v8, %[lb]\n"
        "v_mov_b32 v9, 1\n"
        "s_mov_b32 %[cnt], 0x100000\n"
        "Lw0%=:\n"
        "ds_read_b32 v10, v8 offset:12\n"
        "s_sub_u32 %[cnt], %[cnt], 1\n"
        "s_cbranch_scc1 Lw0x%=\n"
        "s_waitcnt lgkmcnt(0)\n"
        "v_cmp_gt_u32 vcc, 4, v10\n"
        "s_cbranch_vccz Lw0x%=\n"
        "s_sleep 1\n"
        "s_branch Lw0%=\n"
        "Lw0x%=:\n"
        "ds_read_b64_tr_b16 v[64:65], v1 offset:0\n"
        "ds_read_b64_tr_b16 v[66:67], v1 offset:512\n"
        "ds_read_b64_tr_b16 v[68:69], v1 offset:2048\n"
        "ds_read_b64_tr_b16 v[70:71], v1 offset:2560\n"
        "ds_read_b64_tr_b16 v[72:73], v1 offset:4096\n"
        "ds_read_b64_tr_b16 v[74:75], v1 offset:4608\n"
        "ds_read_b64_tr_b16 v[76:77], v1 offset:6144\n"
        "ds_read_b64_tr_b16 v[78:79], v1 offset:6656\n"
        "ds_read_b64_tr_b16 v[80:81], v1 offset:8192\n"
        "ds_read_b64_tr_b16 v[82:83], v1 offset:8704\n"
        "ds_read_b64_tr_b16 v[84:85], v1 offset:10240\n"
        "ds_read_b64_tr_b16 v[86:87], v1 offset:10752\n"
        "ds_read_b64_tr_b16 v[88:89], v1 offset:12288\n"
        "ds_read_b64_tr_b16 v[90:91], v1 offset:12800\n"
        "ds_read_b64_tr_b16 v[92:93], v1 offset:14336\n"
        "ds_read_b64_tr_b16 v[94:95], v1 offset:14848\n"
        "ds_read_b64_tr_b16 v[96:97], v1 offset:16384\n"
        "ds_read_b64_tr_b16 v[98:99], v1 offset:16896\n"
        "ds_read_b64_tr_b16 v[100:101], v1 offset:18432\n"
        "ds_read_b64_tr_b16 v[102:103], v1 offset:18944\n"
        "ds_read_b64_tr_b16 v[104:105], v1 offset:20480\n"
        "ds_read_b64_tr_b16 v[106:107], v1 offset:20992\n"
        "ds_read_b64_tr_b16 v[108:109], v1 offset:22528\n"
        "ds_read_b64_tr_b16 v[110:111], v1 offset:23040\n"
        "ds_read_b64_tr_b16 v[112:113], v1 offset:24576\n"
        "ds_read_b64_tr_b16 v[114:115], v1 offset:25088\n"
        "ds_read_b64_tr_b16 v[116:117], v1 offset:26624\n"
        "ds_read_b64_tr_b16 v[118:119], v1 offset:27136\n"
        "ds_read_b64_tr_b16 v[120:121], v1 offset:28672\n"
        "ds_read_b64_tr_b16 v[122:123], v1 offset:29184\n"
        "ds_read_b64_tr_b16 v[124:125], v1 offset:30720\n"
        "ds_read_b64_tr_b16 v[126:127], v1 offset:31232\n"
        "s_mov_b64 exec, 1\n"
        "ds_add_u32 v8, v9 offset:20\n"
        "s_mov_b64 exec, -1\n"
        "s_mov_b32 %[cnt], 0x100000\n"
        "Lw1%=:\n"
        "ds_read_b32 v10, v8 offset:20\n"
        "s_sub_u32 %[cnt], %[cnt], 1\n"
        "s_cbranch_scc1 Lw1x%=\n"
        "s_waitcnt lgkmcnt(0)\n"
        "v_cmp_gt_u32 vcc, 4, v10\n"
        "s_cbranch_vccz Lw1x%=\n"
        "s_sleep 1\n"
        "s_branch Lw1%=\n"
        "Lw1x%=:\n"
        "s_mov_b32 %[cnt], 0x100000\n"
        "Lw2%=:\n"
        "ds_read_b32 v10, v8 offset:28\n"
        "s_sub_u32 %[cnt], %[cnt], 1\n"
        "s_cbranch_scc1 Lw2x%=\n"
        "s_waitcnt lgkmcnt(0)\n"
        "v_cmp_gt_u32 vcc, 1, v10\n"
        "s_cbranch_vccz Lw2x%=\n"
        "s_sleep 1\n"
        "s_branch Lw2%=\n"
        "Lw2x%=:\n"
        "s_mov_b32 %[i], 0\n"
        "Lld%=:\n"
        "s_mov_b32 %[cnt], 0x100000\n"
        "Lsp%=:\n"
        "ds_read_b32 v1, v2\n"
        "s_sub_u32 %[cnt], %[cnt], 1\n"
        "s_cbranch_scc1 Lspx%=\n"
        "s_waitcnt lgkmcnt(0)\n"
        "v_cmp_ne_u32 vcc, 0, v1\n"
        "s_cbranch_vccz Lsp%=\n"
        "Lspx%=:\n"
        "ds_read_b128 v[12:15], v4 offset:0\n"
        "ds_read_b128 v[16:19], v4 offset:32\n"
        "ds_read_b128 v[20:23], v4 offset:64\n"
        "ds_read_b128 v[24:27], v4 offset:96\n"
        "s_waitcnt lgkmcnt(0)\n"
        "Ltop%=:\n"
        "s_waitcnt lgkmcnt(3)\n"
        "v_mfma_f32_32x32x16_bf16 v[48:63], v[64:67], v[12:15], 0\n"
        "ds_read_b128 v[12:15], v4 offset:128\n"
        "v_add_u32 v8, %[po], v3\n"
        "v_mul_f32 v32, 0xbfb8aa3b, v32\n"
        "s_waitcnt lgkmcnt(3)\n"
        "v_mfma_f32_32x32x16_bf16 v[48:63], v[68:71], v[16:19], v[48:63]\n"
        "ds_read_b128 v[16:19], v4 offset:160\n"
        "v_mul_f32 v33, 0xbfb8aa3b, v33\n"
        "v_mul_f32 v34, 0xbfb8aa3b, v34\n"
        "s_waitcnt lgkmcnt(3)\n"
        "v_mfma_f32_32x32x16_bf16 v[48:63], v[72:75], v[20:23], v[48:63]\n"
        "ds_read_b128 v[20:23], v4 offset:192\n"
        "v_mul_f32 v35, 0xbfb8aa3b, v35\n"
        "v_mul_f32 v36, 0xbfb8aa3b, v36\n"
        "s_waitcnt lgkmcnt(3)\n"
        "v_mfma_f32_32x32x16_bf16 v[48:63], v[76:79], v[24:27], v[48:63]\n"
        "ds_read_b128 v[24:27], v4 offset:224\n"
        "v_mul_f32 v37, 0xbfb8aa3b, v37\n"
        "v_mul_f32 v38, 0xbfb8aa3b, v38\n"
        "s_waitcnt lgkmcnt(3)\n"
        "v_mfma_f32_32x32x16_bf16 v[48:63], v[80:83], v[12:15], v[48:63]\n"
        "ds_read_b128 v[12:15], v4 offset:256\n"
        "v_mul_f32 v39, 0xbfb8aa3b, v39\n"
        "v_cvt_pk_bf16_f32 v40, v40, v41\n"
        "s_waitcnt lgkmcnt(3)\n"
        "v_mfma_f32_32x32x16_bf16 v[48:63], v[84:87], v[16:19], v[48:63]\n"
        "ds_read_b128 v[16:19], v4 offset:288\n"
        "v_cvt_pk_bf16_f32 v41, v42, v43\n"
        "ds_write_b64 v8, v[40:41] offset:128\n"
        "s_waitcnt lgkmcnt(4)\n"
        "v_mfma_f32_32x32x16_bf16 v[48:63], v[88:91], v[20:23], v[48:63]\n"
        "ds_read_b128 v[20:23], v4 offset:320\n"
        "v_cvt_pk_bf16_f32 v44, v44, v45\n"
        "v_cvt_pk_bf16_f32 v45, v46, v47\n"
        "s_waitcnt lgkmcnt(4)\n"
        "v_mfma_f32_32x32x16_bf16 v[48:63], v[92:95], v[24:27], v[48:63]\n"
        "ds_read_b128 v[24:27], v4 offset:352\n"
        "ds_write_b64 v8, v[44:45] offset:144\n"
        "v_cvt_pk_bf16_f32 v32, v32, v33\n"
        "s_waitcnt lgkmcnt(5)\n"
        "v_mfma_f32_32x32x16_bf16 v[48:63], v[96:99], v[12:15], v[48:63]\n"
        "ds_read_b128 v[12:15], v4 offset:384\n"
        "v_cvt_pk_bf16_f32 v33, v34, v35\n"
        "ds_write_b64 v8, v[32:33] offset:0\n"
        "s_waitcnt lgkmcnt(6)\n"
        "v_mfma_f32_32x32x16_bf16 v[48:63], v[100:103], v[16:19], v[48:63]\n"
        "ds_read_b128 v[16:19], v4 offset:416\n"
        "v_cvt_pk_bf16_f32 v36, v36, v37\n"
        "v_cvt_pk_bf16_f32 v37, v38, v39\n"
        "s_waitcnt lgkmcnt(5)\n"
        "v_mfma_f32_32x32x16_bf16 v[48:63], v[104:107], v[20:23], v[48:63]\n"
        "ds_read_b128 v[20:23], v4 offset:448\n"
        "ds_write_b64 v8, v[36:37] offset:16\n"
        "s_waitcnt lgkmcnt(6)\n"
        "v_mfma_f32_32x32x16_bf16 v[48:63], v[108:111], v[24:27], v[48:63]\n"
        "ds_read_b128 v[24:27], v4 offset:480\n"
        "s_waitcnt lgkmcnt(5)\n"
        "v_mfma_f32_32x32x16_bf16 v[48:63], v[112:115], v[12:15], v[48:63]\n"
        "ds_read_b32 v1, v2 offset:4\n"
        "ds_read_b128 v[12:15], v4 offset:16896\n"
        "s_waitcnt lgkmcnt(5)\n"
        "v_mfma_f32_32x32x16_bf16 v[48:63], v[116:119], v[16:19], v[48:63]\n"
        "ds_read_b128 v[16:19], v4 offset:16928\n"
        "s_waitcnt lgkmcnt(5)\n"
        "v_mfma_f32_32x32x16_bf16 v[48:63], v[120:123], v[20:23], v[48:63]\n"
        "ds_read_b128 v[20:23], v4 offset:16960\n"
        "s_waitcnt lgkmcnt(4)\n"
        "v_mfma_f32_32x32x16_bf16 v[32:47], v[124:127], v[24:27], v[48:63]\n"
        "ds_read_b128 v[24:27], v4 offset:16992\n"
        "s_mov_b32 %[po], %[no]\n"
        "s_add_u32 %[no], %[no], 8448\n"
        "s_cmp_eq_u32 %[no], 42240\n"
        "s_cselect_b32 %[no], 0, %[no]\n"
        "s_add_u32 %[i], %[i], 1\n"
        "v_add_u32 v2, 4, v2\n"
        "v_add_u32 v4, 16896, v4\n"
        "s_waitcnt lgkmcnt(4)\n"
        "v_cmp_ne_u32 vcc, 0, v1\n"
        "s_cbranch_vccnz Ltop%=\n"
        "s_nop 2\n"
        "v_add_u32 v8, %[po], v3\n"
        "v_mul_f32 v32, 0xbfb8aa3b, v32\n"
        "v_mul_f32 v33, 0xbfb8aa3b, v33\n"
        "v_mul_f32 v34, 0xbfb8aa3b, v34\n"
        "v_mul_f32 v35, 0xbfb8aa3b, v35\n"
        "v_mul_f32 v36, 0xbfb8aa3b, v36\n"
        "v_mul_f32 v37, 0xbfb8aa3b, v37\n"
        "v_mul_f32 v38, 0xbfb8aa3b, v38\n"
        "v_mul_f32 v39, 0xbfb8aa3b, v39\n"
        "v_cvt_pk_bf16_f32 v40, v40, v41\n"
        "v_cvt_pk_bf16_f32 v41, v42, v43\n"
        "ds_write_b64 v8, v[40:41] offset:128\n"
        "v_cvt_pk_bf16_f32 v44, v44, v45\n"
        "v_cvt_pk_bf16_f32 v45, v46, v47\n"
        "ds_write_b64 v8, v[44:45] offset:144\n"
        "v_cvt_pk_bf16_f32 v32, v32, v33\n"
        "v_cvt_pk_bf16_f32 v33, v34, v35\n"
        "ds_write_b64 v8, v[32:33] offset:0\n"
        "v_cvt_pk_bf16_f32 v36, v36, v37\n"
        "v_cvt_pk_bf16_f32 v37, v38, v39\n"
        "ds_write_b64 v8, v[36:37] offset:16\n"
        "s_mov_b32 %[po], %[no]\n"
        "s_cmp_lt_u32 %[i], 5\n"
        "s_cbranch_scc1 Lld%=\n"
        "s_waitcnt lgkmcnt(0)\n"
          : [i] "=&s"(gi), [cnt] "=&s"(gc), [no] "+&s"(no), [po] "+&s"(po), "+{v1}"(wt), "+{v2}"(fl), "+{v4}"(xr)
          : [lb] "s"((u32)(size_t)S), "{v3}"((u32)(size_t)(S + L_XA) + n * AUP + cg * 32 + h * 8)
          : "memory", "scc", "vcc", "v6", "v7", "v8", "v9", "v10", "v12", "v13", "v14", "v15", "v16", "v17", "v18", "v19", "v20", "v21", "v22", "v23", "v24", "v25", "v26", "v27", "v32", "v33", "v34", "v35", "v36", "v37", "v38", "v39", "v40", "v41", "v42", "v43", "v44", "v45", "v46", "v47", "v48", "v49", "v50", "v51", "v52", "v53", "v54", "v55", "v56", "v57", "v58", "v59", "v60", "v61", "v62", "v63", "v64", "v65", "v66", "v67", "v68", "v69", "v70", "v71", "v72", "v73", "v74", "v75", "v76", "v77", "v78", "v79", "v80", "v81", "v82", "v83", "v84", "v85", "v86", "v87", "v88", "v89", "v90", "v91", "v92", "v93", "v94", "v95", "v96", "v97", "v98", "v99", "v100", "v101", "v102", "v103", "v104", "v105", "v106", "v107", "v108", "v109", "v110", "v111", "v112", "v113", "v114", "v115", "v116", "v117", "v118", "v119", "v120", "v121", "v122", "v123", "v124", "v125", "v126", "v127");
    } else {
      __builtin_amdgcn_s_setprio(2);
      const auto trs = RSRC(p.T + ((size_t)s * p.N + n0) * 64, 0, nrows * 256);
      GSETUP();
      u32 no = rot * 8192, po = OOB;
      asm volatile(
        "v_mov_b32 v8, %[lb]\n"
        "v_mov_b32 v9, 1\n"
        "s_mov_b32 %[cnt], 0x100000\n"
        "Lw0%=:\n"
        "ds_read_b32 v10, v8 offset:8\n"
        "s_sub_u32 %[cnt], %[cnt], 1\n"
        "s_cbranch_scc1 Lw0x%=\n"
        "s_waitcnt lgkmcnt(0)\n"
        "v_cmp_gt_u32 vcc, 8, v10\n"
        "s_cbranch_vccz Lw0x%=\n"
        "s_sleep 1\n"
        "s_branch Lw0%=\n"
        "Lw0x%=:\n"
        "ds_read_b64_tr_b16 v[64:65], v1 offset:0\n"
        "ds_read_b64_tr_b16 v[66:67], v1 offset:512\n"
        "ds_read_b64_tr_b16 v[68:69], v1 offset:2048\n"
        "ds_read_b64_tr_b16 v[70:71], v1 offset:2560\n"
        "ds_read_b64_tr_b16 v[72:73], v1 offset:4096\n"
        "ds_read_b64_tr_b16 v[74:75], v1 offset:4608\n"
        "ds_read_b64_tr_b16 v[76:77], v1 offset:6144\n"
        "ds_read_b64_tr_b16 v[78:79], v1 offset:6656\n"
        "ds_read_b64_tr_b16 v[80:81], v1 offset:8192\n"
        "ds_read_b64_tr_b16 v[82:83], v1 offset:8704\n"
        "ds_read_b64_tr_b16 v[84:85], v1 offset:10240\n"
        "ds_read_b64_tr_b16 v[86:87], v1 offset:10752\n"
        "ds_read_b64_tr_b16 v[88:89], v1 offset:12288\n"
        "ds_read_b64_tr_b16 v[90:91], v1 offset:12800\n"
        "ds_read_b64_tr_b16 v[92:93], v1 offset:14336\n"
        "ds_read_b64_tr_b16 v[94:95], v1 offset:14848\n"
        "ds_read_b64_tr_b16 v[96:97], v1 offset:16384\n"
        "ds_read_b64_tr_b16 v[98:99], v1 offset:16896\n"
        "ds_read_b64_tr_b16 v[100:101], v1 offset:18432\n"
        "ds_read_b64_tr_b16 v[102:103], v1 offset:18944\n"
        "ds_read_b64_tr_b16 v[104:105], v1 offset:20480\n"
        "ds_read_b64_tr_b16 v[106:107], v1 offset:20992\n"
        "ds_read_b64_tr_b16 v[108:109], v1 offset:22528\n"
        "ds_read_b64_tr_b16 v[110:111], v1 offset:23040\n"
        "ds_read_b64_tr_b16 v[112:113], v1 offset:24576\n"
        "ds_read_b64_tr_b16 v[114:115], v1 offset:25088\n"
        "ds_read_b64_tr_b16 v[116:117], v1 offset:26624\n"
        "ds_read_b64_tr_b16 v[118:119], v1 offset:27136\n"
        "ds_read_b64_tr_b16 v[120:121], v1 offset:28672\n"
        "ds_read_b64_tr_b16 v[122:123], v1 offset:29184\n"
        "ds_read_b64_tr_b16 v[124:125], v1 offset:30720\n"
        "ds_read_b64_tr_b16 v[126:127], v1 offset:31232\n"
        "s_mov_b64 exec, 1\n"
        "ds_add_u32 v8, v9 offset:16\n"
        "s_mov_b64 exec, -1\n"
        "s_mov_b32 %[i], 0\n"
        "Lld%=:\n"
        "s_mov_b32 %[cnt], 0x100000\n"
        "Lsp%=:\n"
        "ds_read_b32 v1, v2\n"
        "s_sub_u32 %[cnt], %[cnt], 1\n"
        "s_cbranch_scc1 Lspx%=\n"
        "s_waitcnt lgkmcnt(0)\n"
        "v_cmp_ne_u32 vcc, 0, v1\n"
        "s_cbranch_vccz Lsp%=\n"
        "Lspx%=:\n"
        "ds_read_b128 v[12:15], v4 offset:0\n"
        "ds_read_b128 v[16:19], v4 offset:32\n"
        "ds_read_b128 v[20:23], v4 offset:64\n"
        "ds_read_b128 v[24:27], v4 offset:96\n"
        "s_waitcnt lgkmcnt(0)\n"
        "Ltop%=:\n"
        "s_waitcnt lgkmcnt(3)\n"
        "v_mfma_f32_32x32x16_bf16 v[48:63], v[64:67], v[12:15], 0\n"
        "ds_read_b128 v[12:15], v4 offset:128\n"
        "v_cvt_pk_bf16_f32 v6, v40, v42\n"
        "v_cvt_pk_bf16_f32 v7, v44, v46\n"
        "v_fmamk_f32 v32, v32, 0xbfb8aa3b, v5\n"
        "v_fmamk_f32 v33, v33, 0xbfb8aa3b, v5\n"
        "s_waitcnt lgkmcnt(3)\n"
        "v_mfma_f32_32x32x16_bf16 v[48:63], v[68:71], v[16:19], v[48:63]\n"
        "ds_read_b128 v[16:19], v4 offset:160\n"
        "v_fmamk_f32 v34, v34, 0xbfb8aa3b, v5\n"
        "v_fmamk_f32 v35, v35, 0xbfb8aa3b, v5\n"
        "v_add_f32 v41, v5, v41\n"
        "v_add_f32 v43, v5, v43\n"
        "s_waitcnt lgkmcnt(3)\n"
        "v_mfma_f32_32x32x16_bf16 v[48:63], v[72:75], v[20:23], v[48:63]\n"
        "ds_read_b128 v[20:23], v4 offset:192\n"
        "v_fmamk_f32 v36, v36, 0xbfb8aa3b, v5\n"
        "v_fmamk_f32 v37, v37, 0xbfb8aa3b, v5\n"
        "v_fmamk_f32 v38, v38, 0xbfb8aa3b, v5\n"
        "v_fmamk_f32 v39, v39, 0xbfb8aa3b, v5\n"
        "s_waitcnt lgkmcnt(3)\n"
        "v_mfma_f32_32x32x16_bf16 v[48:63], v[76:79], v[24:27], v[48:63]\n"
        "ds_read_b128 v[24:27], v4 offset:224\n"
        "v_add_f32 v45, v5, v45\n"
        "v_add_f32 v47, v5, v47\n"
        "v_add_u32 v32, 0x8000, v32\n"
        "v_add_u32 v33, 0x8000, v33\n"
        "s_waitcnt lgkmcnt(3)\n"
        "v_mfma_f32_32x32x16_bf16 v[48:63], v[80:83], v[12:15], v[48:63]\n"
        "ds_read_b128 v[12:15], v4 offset:256\n"
        "v_add_u32 v34, 0x8000, v34\n"
        "v_add_u32 v35, 0x8000, v35\n"
        "v_add_u32 v41, 0x8000, v41\n"
        "v_add_u32 v43, 0x8000, v43\n"
        "s_waitcnt lgkmcnt(3)\n"
        "v_mfma_f32_32x32x16_bf16 v[48:63], v[84:87], v[16:19], v[48:63]\n"
        "ds_read_b128 v[16:19], v4 offset:288\n"
        "v_add_u32 v36, 0x8000, v36\n"
        "v_add_u32 v37, 0x8000, v37\n"
        "v_add_u32 v38, 0x8000, v38\n"
        "v_add_u32 v39, 0x8000, v39\n"
        "s_waitcnt lgkmcnt(3)\n"
        "v_mfma_f32_32x32x16_bf16 v[48:63], v[88:91], v[20:23], v[48:63]\n"
        "ds_read_b128 v[20:23], v4 offset:320\n"
        "v_add_u32 v45, 0x8000, v45\n"
        "v_add_u32 v47, 0x8000, v47\n"
        "v_sub_u32_sdwa v32, v32, v6 clamp dst_sel:DWORD dst_unused:UNUSED_PAD src0_sel:DWORD src1_sel:WORD_0\n"
        "v_sub_u32_sdwa v34, v34, v6 clamp dst_sel:DWORD dst_unused:UNUSED_PAD src0_sel:DWORD src1_sel:WORD_1\n"
        "s_waitcnt lgkmcnt(3)\n"
        "v_mfma_f32_32x32x16_bf16 v[48:63], v[92:95], v[24:27], v[48:63]\n"
        "ds_read_b128 v[24:27], v4 offset:352\n"
        "v_sub_u32_sdwa v36, v36, v7 clamp dst_sel:DWORD dst_unused:UNUSED_PAD src0_sel:DWORD src1_sel:WORD_0\n"
        "v_sub_u32_sdwa v38, v38, v7 clamp dst_sel:DWORD dst_unused:UNUSED_PAD src0_sel:DWORD src1_sel:WORD_1\n"
        "v_sub_u32_sdwa v41, v41, v32 clamp dst_sel:DWORD dst_unused:UNUSED_PAD src0_sel:DWORD src1_sel:WORD_1\n"
        "v_sub_u32_sdwa v43, v43, v34 clamp dst_sel:DWORD dst_unused:UNUSED_PAD src0_sel:DWORD src1_sel:WORD_1\n"
        "s_waitcnt lgkmcnt(3)\n"
        "v_mfma_f32_32x32x16_bf16 v[48:63], v[96:99], v[12:15], v[48:63]\n"
        "ds_read_b128 v[12:15], v4 offset:384\n"
        "v_sub_u32_sdwa v45, v45, v36 clamp dst_sel:DWORD dst_unused:UNUSED_PAD src0_sel:DWORD src1_sel:WORD_1\n"
        "v_sub_u32_sdwa v47, v47, v38 clamp dst_sel:DWORD dst_unused:UNUSED_PAD src0_sel:DWORD src1_sel:WORD_1\n"
        "v_perm_b32 v32, v32, v6, %[p0]\n"
        "v_perm_b32 v34, v34, v6, %[p1]\n"
        "s_waitcnt lgkmcnt(3)\n"
        "v_mfma_f32_32x32x16_bf16 v[48:63], v[100:103], v[16:19], v[48:63]\n"
        "ds_read_b128 v[16:19], v4 offset:416\n"
        "v_perm_b32 v36, v36, v7, %[p0]\n"
        "v_perm_b32 v38, v38, v7, %[p1]\n"
        "v_sub_u32_sdwa v33, v33, v41 clamp dst_sel:DWORD dst_unused:UNUSED_PAD src0_sel:DWORD src1_sel:WORD_1\n"
        "v_sub_u32_sdwa v35, v35, v43 clamp dst_sel:DWORD dst_unused:UNUSED_PAD src0_sel:DWORD src1_sel:WORD_1\n"
        "s_waitcnt lgkmcnt(3)\n"
        "v_mfma_f32_32x32x16_bf16 v[48:63], v[104:107], v[20:23], v[48:63]\n"
        "ds_read_b128 v[20:23], v4 offset:448\n"
        "v_sub_u32_sdwa v37, v37, v45 clamp dst_sel:DWORD dst_unused:UNUSED_PAD src0_sel:DWORD src1_sel:WORD_1\n"
        "v_sub_u32_sdwa v39, v39, v47 clamp dst_sel:DWORD dst_unused:UNUSED_PAD src0_sel:DWORD src1_sel:WORD_1\n"
        "v_permlane16_swap_b32 v32, v36\n"
        "v_permlane16_swap_b32 v34, v38\n"
        "s_waitcnt lgkmcnt(3)\n"
        "v_mfma_f32_32x32x16_bf16 v[48:63], v[108:111], v[24:27], v[48:63]\n"
        "ds_read_b128 v[24:27], v4 offset:480\n"
        "v_perm_b32 v33, v33, v41, %[p1]\n"
        "v_perm_b32 v37, v37, v45, %[p1]\n"
        "v_perm_b32 v35, v35, v43, %[p1]\n"
        "v_perm_b32 v39, v39, v47, %[p1]\n"
        "s_waitcnt lgkmcnt(3)\n"
        "v_mfma_f32_32x32x16_bf16 v[48:63], v[112:115], v[12:15], v[48:63]\n"
        "ds_read_b32 v1, v2 offset:4\n"
        "ds_read_b128 v[12:15], v4 offset:16896\n"
        "v_permlane16_swap_b32 v33, v37\n"
        "s_nop 0\n"
        "v_permlane16_swap_b32 v35, v39\n"
        "s_add_u32 %[cnt], %[po], 4096\n"
        "s_waitcnt lgkmcnt(4)\n"
        "v_mfma_f32_32x32x16_bf16 v[48:63], v[116:119], v[16:19], v[48:63]\n"
        "ds_read_b128 v[16:19], v4 offset:16928\n"
        "s_cmp_eq_u32 %[po], 0x7f000000\n"
        "s_cbranch_scc1 Lvs%=\n"
        "buffer_store_dwordx4 v[32:35], v3, %[trs], %[po] offen sc1\n"
        "buffer_store_dwordx4 v[36:39], v3, %[trs], %[cnt] offen sc1\n"
        "Lvs%=:\n"
        "s_waitcnt lgkmcnt(4)\n"
        "v_mfma_f32_32x32x16_bf16 v[48:63], v[120:123], v[20:23], v[48:63]\n"
        "ds_read_b128 v[20:23], v4 offset:16960\n"
        "s_waitcnt lgkmcnt(4)\n"
        "v_mfma_f32_32x32x16_bf16 v[32:47], v[124:127], v[24:27], v[48:63]\n"
        "ds_read_b128 v[24:27], v4 offset:16992\n"
        "s_mov_b32 %[po], %[no]\n"
        "s_add_u32 %[no], %[no], 8192\n"
        "s_cmp_eq_u32 %[no], 40960\n"
        "s_cselect_b32 %[no], 0, %[no]\n"
        "s_add_u32 %[i], %[i], 1\n"
        "v_add_u32 v2, 4, v2\n"
        "v_add_u32 v4, 16896, v4\n"
        "s_waitcnt lgkmcnt(4)\n"
        "v_cmp_ne_u32 vcc, 0, v1\n"
        "s_cbranch_vccnz Ltop%=\n"
        "s_nop 2\n"
        "v_cvt_pk_bf16_f32 v6, v40, v42\n"
        "v_cvt_pk_bf16_f32 v7, v44, v46\n"
        "v_fmamk_f32 v32, v32, 0xbfb8aa3b, v5\n"
        "v_fmamk_f32 v33, v33, 0xbfb8aa3b, v5\n"
        "v_fmamk_f32 v34, v34, 0xbfb8aa3b, v5\n"
        "v_fmamk_f32 v35, v35, 0xbfb8aa3b, v5\n"
        "v_add_f32 v41, v5, v41\n"
        "v_add_f32 v43, v5, v43\n"
        "v_fmamk_f32 v36, v36, 0xbfb8aa3b, v5\n"
        "v_fmamk_f32 v37, v37, 0xbfb8aa3b, v5\n"
        "v_fmamk_f32 v38, v38, 0xbfb8aa3b, v5\n"
        "v_fmamk_f32 v39, v39, 0xbfb8aa3b, v5\n"
        "v_add_f32 v45, v5, v45\n"
        "v_add_f32 v47, v5, v47\n"
        "v_add_u32 v32, 0x8000, v32\n"
        "v_add_u32 v33, 0x8000, v33\n"
        "v_add_u32 v34, 0x8000, v34\n"
        "v_add_u32 v35, 0x8000, v35\n"
        "v_add_u32 v41, 0x8000, v41\n"
        "v_add_u32 v43, 0x8000, v43\n"
        "v_add_u32 v36, 0x8000, v36\n"
        "v_add_u32 v37, 0x8000, v37\n"
        "v_add_u32 v38, 0x8000, v38\n"
        "v_add_u32 v39, 0x8000, v39\n"
        "v_add_u32 v45, 0x8000, v45\n"
        "v_add_u32 v47, 0x8000, v47\n"
        "v_sub_u32_sdwa v32, v32, v6 clamp dst_sel:DWORD dst_unused:UNUSED_PAD src0_sel:DWORD src1_sel:WORD_0\n"
        "v_sub_u32_sdwa v34, v34, v6 clamp dst_sel:DWORD dst_unused:UNUSED_PAD src0_sel:DWORD src1_sel:WORD_1\n"
        "v_sub_u32_sdwa v36, v36, v7 clamp dst_sel:DWORD dst_unused:UNUSED_PAD src0_sel:DWORD src1_sel:WORD_0\n"
        "v_sub_u32_sdwa v38, v38, v7 clamp dst_sel:DWORD dst_unused:UNUSED_PAD src0_sel:DWORD src1_sel:WORD_1\n"
        "v_sub_u32_sdwa v41, v41, v32 clamp dst_sel:DWORD dst_unused:UNUSED_PAD src0_sel:DWORD src1_sel:WORD_1\n"
        "v_sub_u32_sdwa v43, v43, v34 clamp dst_sel:DWORD dst_unused:UNUSED_PAD src0_sel:DWORD src1_sel:WORD_1\n"
        "v_sub_u32_sdwa v45, v45, v36 clamp dst_sel:DWORD dst_unused:UNUSED_PAD src0_sel:DWORD src1_sel:WORD_1\n"
        "v_sub_u32_sdwa v47, v47, v38 clamp dst_sel:DWORD dst_unused:UNUSED_PAD src0_sel:DWORD src1_sel:WORD_1\n"
        "v_perm_b32 v32, v32, v6, %[p0]\n"
        "v_perm_b32 v34, v34, v6, %[p1]\n"
        "v_perm_b32 v36, v36, v7, %[p0]\n"
        "v_perm_b32 v38, v38, v7, %[p1]\n"
        "v_sub_u32_sdwa v33, v33, v41 clamp dst_sel:DWORD dst_unused:UNUSED_PAD src0_sel:DWORD src1_sel:WORD_1\n"
        "v_sub_u32_sdwa v35, v35, v43 clamp dst_sel:DWORD dst_unused:UNUSED_PAD src0_sel:DWORD src1_sel:WORD_1\n"
        "v_sub_u32_sdwa v37, v37, v45 clamp dst_sel:DWORD dst_unused:UNUSED_PAD src0_sel:DWORD src1_sel:WORD_1\n"
        "v_sub_u32_sdwa v39, v39, v47 clamp dst_sel:DWORD dst_unused:UNUSED_PAD src0_sel:DWORD src1_sel:WORD_1\n"
        "v_permlane16_swap_b32 v32, v36\n"
        "v_permlane16_swap_b32 v34, v38\n"
        "v_perm_b32 v33, v33, v41, %[p1]\n"
        "v_perm_b32 v37, v37, v45, %[p1]\n"
        "v_perm_b32 v35, v35, v43, %[p1]\n"
        "v_perm_b32 v39, v39, v47, %[p1]\n"
        "v_permlane16_swap_b32 v33, v37\n"
        "s_nop 0\n"
        "v_permlane16_swap_b32 v35, v39\n"
        "s_add_u32 %[cnt], %[po], 4096\n"
        "buffer_store_dwordx4 v[32:35], v3, %[trs], %[po] offen sc1\n"
        "buffer_store_dwordx4 v[36:39], v3, %[trs], %[cnt] offen sc1\n"
        "s_mov_b32 %[po], 0x7f000000\n"
        "s_cmp_lt_u32 %[i], 5\n"
        "s_cbranch_scc1 Lld%=\n"
        "s_waitcnt lgkmcnt(0)\n"
          : [i] "=&s"(gi), [cnt] "=&s"(gc), [no] "+&s"(no), [po] "+&s"(po), "+{v1}"(wt), "+{v2}"(fl), "+{v4}"(xr)
          : [lb] "s"((u32)(size_t)S), [trs] "s"(trs), [p0] "s"(0x07060100u), [p1] "s"(0x07060302u),
            "{v3}"(j * 256 + cg * 64 + (h | (q & 1) << 1) * 16), "{v5}"(TINY)
          : "memory", "scc", "vcc", "v6", "v7", "v8", "v9", "v10", "v12", "v13", "v14", "v15", "v16", "v17", "v18", "v19", "v20", "v21", "v22", "v23", "v24", "v25", "v26", "v27", "v32", "v33", "v34", "v35", "v36", "v37", "v38", "v39", "v40", "v41", "v42", "v43", "v44", "v45", "v46", "v47", "v48", "v49", "v50", "v51", "v52", "v53", "v54", "v55", "v56", "v57", "v58", "v59", "v60", "v61", "v62", "v63", "v64", "v65", "v66", "v67", "v68", "v69", "v70", "v71", "v72", "v73", "v74", "v75", "v76", "v77", "v78", "v79", "v80", "v81", "v82", "v83", "v84", "v85", "v86", "v87", "v88", "v89", "v90", "v91", "v92", "v93", "v94", "v95", "v96", "v97", "v98", "v99", "v100", "v101", "v102", "v103", "v104", "v105", "v106", "v107", "v108", "v109", "v110", "v111", "v112", "v113", "v114", "v115", "v116", "v117", "v118", "v119", "v120", "v121", "v122", "v123", "v124", "v125", "v126", "v127");
      WAITGE(C_PDONE, 1);
      const u64 tag = *(volatile LAS u64*)&ctrl[C_TAGLO];
      const bool l0 = fresh_lane() == 0;
      asm volatile("s_waitcnt vmcnt(0)" :: "v"(tag) : "memory");
      u32 old = 0;
      if (l0) old = LDS_ADD(&ctrl[C_TDONE], 1u);
      if (l0 && old == 3) ST_SC1(p.tflag + (s * NB + beta) * 8, tag);
    }
    SIGNAL(C_LOCAL);
  } else {
    __builtin_amdgcn_s_setprio(3);
    u64 tag;
    if (w == 2) {
      const int e0 = b * p.EPP, ne = max(min(p.EPP, p.E - e0), 0);
      const auto drs = RSRC(p.dst + e0, 0, ne * 4);
      const auto srs = RSRC(p.src + e0, 0, ne * 4);
      const auto ers = RSRC(p.ea + (size_t)e0 * 2, 0, ne * 8);
      u32 dd[10], ss[10];
      u32x2 aa[10];
#pragma unroll
      for (int i = 0; i < 10; i++) dd[i] = __builtin_amdgcn_raw_buffer_load_b32(drs, lane * 4, i * 256, 0);
#pragma unroll
      for (int i = 0; i < 10; i++) ss[i] = __builtin_amdgcn_raw_buffer_load_b32(srs, lane * 4, i * 256, 0);
#pragma unroll
      for (int i = 0; i < 10; i++) aa[i] = __builtin_amdgcn_raw_buffer_load_b64(ers, lane * 8, i * 512, 0);
      const u64 g0 = LD_SC1(p.gen);
      __builtin_amdgcn_s_barrier();
      u32 bk[10], rk[10];
#pragma unroll
      for (int i = 0; i < 10; i++) {
        bk[i] = min(__umulhi(dd[i], p.magic), 63u);
        rk[i] = EPPMAX;
        if (i * 64 + lane < ne) rk[i] = LDS_ADD(S + L_HIST1 + bk[i] * 4, 1u);
      }
      CBAR();
      const u32 c = *(volatile LAS u32*)(S + L_HIST1 + lane * 4);
      const u32 st = scan64(c) - c;
      u32 pos[10];
#pragma unroll
      for (int i = 0; i < 10; i++) pos[i] = __builtin_amdgcn_ds_bpermute(bk[i] * 4, st);
      __builtin_amdgcn_sched_barrier(0);
#pragma unroll
      for (int i = 0; i < 10; i++) {
        LAS u32* d = (LAS u32*)(S + L_STG + min(pos[i] + rk[i], (u32)EPPMAX) * 12);
        d[0] = aa[i].x;
        d[1] = aa[i].y;
        d[2] = ss[i] << 16 | (dd[i] - bk[i] * p.BS) * 4;
      }
      CBAR();
      const auto rrs = RSRC((char*)p.recs + (size_t)b * RECSTRIDE, 0, RECSTRIDE);
      u32x4 v[8];
#pragma unroll
      for (int i = 0; i < 8; i++) v[i] = *(volatile LAS u32x4*)(S + L_STG + i * 1024 + lane * 16);
#pragma unroll
      for (int i = 0; i < 8; i++) __builtin_amdgcn_raw_buffer_store_b128(v[i], rrs, lane * 16, i * 1024, 16);
      tag = g0 + 1;
      *(volatile LAS u64*)&ctrl[C_TAGLO] = tag;
      ctrl[C_PDONE] = 1;
      VMCNT(0);
      ST_SC1(p.gran + b * NB + lane, tag << 20 | st << 10 | c);
      ctrl[C_PDONE] = 2;
    } else {
      u32 pf[3] = {0, 0, 0};
      if (w == 3) {
        u64 pc; u32 off;
        asm volatile("s_getpc_b64 %0\nLpc%=:\n\ts_mov_b32 %1, Lpc%=-_Z5fused6Params" : "=s"(pc), "=s"(off));
        const auto crs = RSRC(pc - off, 0, CODELEN);
#pragma unroll
        for (int k = 0; k < 3; k++) pf[k] = __builtin_amdgcn_raw_buffer_load_b32(crs, lane * 128, k * 8192, 0);
      }
      __builtin_amdgcn_s_barrier();
      asm volatile("" :: "v"(pf[0]), "v"(pf[1]), "v"(pf[2]));
      WAITGE(C_PDONE, 2);
      tag = *(volatile LAS u64*)&ctrl[C_TAGLO];
    }
    const int c = 7 - w;
    const int pid = c * 44 + lane;
    const bool has = lane < 44 && pid < NWG;
    u64 gv = 0;
    {
      const u64* gp = p.gran + min(pid, NWG - 1) * NB + beta;
      bool ok = !has;
#pragma unroll 1
      for (int it = 0; it < SPINCAP; it++) {
        __builtin_amdgcn_s_sleep(32);
        if (!ok) { gv = LD_SC1(gp); ok = (gv ^ tag << 20) >> 20 == 0; }
        if (__all(ok)) break;
      }
      CBAR();
    }
    const u32 cnt = has ? (u32)gv & 1023u : 0u;
    u32 dsc = (u32)(pid * EPPMAX + (((u32)gv >> 10) & 1023u)) << 6 | min(cnt, 16u);
    const u64 big = __ballot(cnt > 16);
    const u32 d2 = __builtin_amdgcn_ds_permute(
        (cnt > 16 ? 44 + __builtin_amdgcn_mbcnt_hi((u32)(big >> 32), __builtin_amdgcn_mbcnt_lo((u32)big, 0u)) : 0u) * 4,
        ((dsc & ~63u) + (16u << 6)) | min(cnt - 16, 16u));
    const auto rrs = RSRC(p.recs, 0, NWG * RECSTRIDE);
    u32x3 rec[13];
    u32 rk[13];
    u64 msk[13];
#define FORS(i0, i1) _Pragma("unroll") for (int k = i0; k < i1; k++)
#define LIVE __builtin_amdgcn_inverse_ballot_w64(msk[k])
    FORS(0, 11) rk[k] = __builtin_amdgcn_ds_bpermute((k * 4 + q) * 4, dsc);
    __builtin_amdgcn_sched_barrier(0);
    FORS(0, 11) {
      msk[k] = __ballot(j < (rk[k] & 63u));
      rec[k] = __builtin_amdgcn_raw_buffer_load_b96(rrs, LIVE ? ((rk[k] >> 6) + j) * 12 : OOB, 0, 16);
    }
    const int nbig = __builtin_popcountll(big);
    FORS(11, 13) rk[k] = __builtin_amdgcn_ds_bpermute((k * 4 + q) * 4, d2);
    FORS(11, 13) {
      msk[k] = __ballot(j < (rk[k] & 63u) && k * 4 + q - 44 < nbig);
      rec[k] = __builtin_amdgcn_raw_buffer_load_b96(rrs, LIVE ? ((rk[k] >> 6) + j) * 12 : OOB, 0, 16);
    }
    if (nbig > 8 || __any(cnt > 32)) ctrl[C_GEN] = 1;
    LAS char* cbase = S + L_CNT + c * 640;
    FORS(0, 13) { rk[k] = 0; if (LIVE) rk[k] = LDS_ADD(cbase + (rec[k].z & 0xffffu), RINC); }
    SIGNAL(C_P1);
    WAITGE(C_LAYOUT, 1);
    if (ctrl[C_GEN] == 0) {
      u32 inf[13];
      FORS(0, 13) inf[k] = *(LAS u32*)(cbase + (rec[k].z & 0xffffu));
      __builtin_amdgcn_sched_barrier(0);
      FORS(0, 13) { rk[k] += inf[k]; asm volatile("" : "+v"(rk[k])); }
      FORS(0, 13) {
        LAS char* ia = S + L_IDX + (rk[k] >> 16);
        LAS char* ea = S + L_EA + 512 + (rk[k] & 0xffffu);
        if (LIVE) { *(LAS u16*)ia = (u16)(rec[k].z >> 16); *(LAS u32x2*)ea = u32x2{rec[k].x, rec[k].y}; }
      }
    }
    LGKM0();
    SIGNAL(C_LOCAL);
  }
  }
  __builtin_amdgcn_s_setprio(3);
  const int lane = fresh_lane(), q = lane >> 4, j = lane & 15, tid = w * 64 + lane;

  u32 cb0, cb1;
  {
    const u16* cw = (const u16*)p.gw + 512 * 256 + s * 64 + j * 4 + q;
    cb0 = pk_bf16(u2f((u32)cw[0] << 16) * NLOG2E, u2f((u32)cw[256] << 16) * NLOG2E);
    cb1 = pk_bf16(u2f((u32)cw[512] << 16) * NLOG2E, u2f((u32)cw[768] << 16) * NLOG2E);
  }
  WAITGE(C_LOCAL, 14);
  const auto ors = RSRC(p.out, 0, p.N * 512);
  const int wq = w ^ 8;
  if (ctrl[C_GEN] == 0) {
    const auto trs = RSRC(p.T + (size_t)s * p.N * 64, 256, p.N);
    const u32x4 wi = *(LAS u32x4*)(S + L_WINFO + wq * 16);
    const int wb = __builtin_amdgcn_readfirstlane(wi.x);
    const int l0 = __builtin_amdgcn_readfirstlane(wi.y), l1 = __builtin_amdgcn_readfirstlane(wi.z),
              l2 = __builtin_amdgcn_readfirstlane(wi.w);
    if (l0 > 0) {
      const bool av = (q == (j & 3));
      u32 v_ea = (u32)(size_t)(S + L_EA) + (av ? 512 + (wb * 4 + (j >> 2)) * 8 : 0);
      const u32 v_eainc = av ? 256u : 0u;
      u32 v_idx = (u32)(size_t)(S + L_IDX) + (wb * 4 + wq * 32 + q) * 2;
      const u32 one = av ? ((j & 4) ? 0x3f800000u : 0x00003f80u) : 0u;
      const u32x2 oh = {(j & 8) ? 0u : one, (j & 8) ? one : 0u};
      u32x2 bq[3];
      u32x4 yq[3];
      u32 oq[3];
#pragma unroll
      for (int k = 0; k < 3; k++) {
        const int qd = k == 0 ? wq : (k == 1 ? 31 - wq : 47 - wq);
        const u32x2 o4 = *(LAS u32x2*)(S + L_ORDER + qd * 8);
        const u32 mine = *(LAS u16*)(S + L_ORDER + qd * 8 + q * 2);
        LAS char* xa = S + L_XA + j * 8 + q * 2;
        u32 a0 = *(LAS u16*)(xa + min(o4.x & 0xffffu, 159u) * AUP), a1 = *(LAS u16*)(xa + min(o4.x >> 16, 159u) * AUP),
            a2 = *(LAS u16*)(xa + min(o4.y & 0xffffu, 159u) * AUP), a3 = *(LAS u16*)(xa + min(o4.y >> 16, 159u) * AUP);
        bq[k] = u32x2{a0 | a1 << 16, a2 | a3 << 16};
        const u32x2 xu = *(LAS u32x2*)(S + L_XU + min(mine, 159u) * AUP + j * 8);
        yq[k] = u32x4{xu.x << 16, xu.x & 0xffff0000u, xu.y << 16, xu.y & 0xffff0000u};
        oq[k] = mine < 160u ? (n0 + mine) * 512u + s * 128 + j * 8 : OOB;
      }
      const u32x2 cb = {cb0, cb1};
      int tot = l0 + l1 + l2, prem = tot, rem, kk;
      u64 ret;
      f32x2 chk = {0.f, 0.f};
      WAITGE(C_TRDY, 1);
      asm volatile(
      "ds_read_u16 v40, v37 offset:0\n"
      "ds_read_u16 v42, v37 offset:8\n"
      "ds_read_u16 v44, v37 offset:16\n"
      "ds_read_u16 v46, v37 offset:24\n"
      "ds_read_u16 v48, v37 offset:32\n"
      "ds_read_u16 v50, v37 offset:40\n"
      "ds_read_u16 v52, v37 offset:48\n"
      "ds_read_u16 v54, v37 offset:56\n"
      "v_mov_b32 v41, v39\n"
      "v_mov_b32 v43, v39\n"
      "v_mov_b32 v45, v39\n"
      "v_mov_b32 v47, v39\n"
      "v_mov_b32 v49, v39\n"
      "v_mov_b32 v51, v39\n"
      "v_mov_b32 v53, v39\n"
      "v_mov_b32 v55, v39\n"
      "s_waitcnt lgkmcnt(0)\n"
      "buffer_load_dwordx4 v[64:67], v[40:41], %[trs], 0 idxen offen sc1\n"
      "buffer_load_dwordx4 v[68:71], v[42:43], %[trs], 0 idxen offen sc1\n"
      "buffer_load_dwordx4 v[72:75], v[44:45], %[trs], 0 idxen offen sc1\n"
      "buffer_load_dwordx4 v[76:79], v[46:47], %[trs], 0 idxen offen sc1\n"
      "buffer_load_dwordx4 v[80:83], v[48:49], %[trs], 0 idxen offen sc1\n"
      "buffer_load_dwordx4 v[84:87], v[50:51], %[trs], 0 idxen offen sc1\n"
      "buffer_load_dwordx4 v[88:91], v[52:53], %[trs], 0 idxen offen sc1\n"
      "buffer_load_dwordx4 v[92:95], v[54:55], %[trs], 0 idxen offen sc1\n"
      "ds_read_b64 v[96:97], v36 offset:0\n"
      "ds_read_u16 v112, v37 offset:64\n"
      "ds_read_b64 v[100:101], v36 offset:32\n"
      "ds_read_u16 v114, v37 offset:72\n"
      "ds_read_b64 v[104:105], v36 offset:64\n"
      "ds_read_u16 v116, v37 offset:80\n"
      "ds_read_b64 v[108:109], v36 offset:96\n"
      "ds_read_u16 v118, v37 offset:88\n"
      "v_mov_b32 v113, v39\n"
      "v_mov_b64 v[98:99], v[126:127]\n"
      "v_mov_b32 v115, v39\n"
      "v_mov_b64 v[102:103], v[126:127]\n"
      "v_mov_b32 v117, v39\n"
      "v_mov_b64 v[106:107], v[126:127]\n"
      "v_mov_b32 v119, v39\n"
      "v_mov_b64 v[110:111], v[126:127]\n"
      "v_mov_b64 v[40:41], 0\n"
      "v_mov_b64 v[42:43], 0\n"
      "v_mov_b64 v[44:45], %[ones]\n"
      "v_mov_b64 v[46:47], %[ones]\n"
      "s_call_b64 %[ret], Lnx0%=\n"
      "s_waitcnt vmcnt(7) lgkmcnt(7)\n"
      "v_mfma_f32_16x16x32_bf16 v[56:59], v[96:99], v[120:123], v[64:67]\n"
      "s_cmp_gt_i32 %[prem], 8\n"
      "s_nop 4\n"
      "s_cbranch_scc1 Lloop%=\n"
      "s_waitcnt vmcnt(3)\n"
      "s_branch Ldr0%=\n"
      "Lloop%=:\n"
      "s_waitcnt vmcnt(6) lgkmcnt(5)\n"
      "v_mfma_f32_16x16x32_bf16 v[60:63], v[100:103], v[120:123], v[68:71]\n"
      "v_exp_f32 v52, v56\n"
      "v_exp_f32 v53, v57\n"
      "v_lshlrev_b64 v[48:49], 16, v[64:65]\n"
      "v_lshlrev_b64 v[50:51], 16, v[66:67]\n"
      "buffer_load_dwordx4 v[64:67], v[112:113], %[trs], 0 idxen offen sc1\n"
      "v_exp_f32 v54, v58\n"
      "v_exp_f32 v55, v59\n"
      "ds_read_b64 v[96:97], v36 offset:128\n"
      "ds_read_u16 v112, v37 offset:96\n"
      "v_pk_fma_f32 v[48:49], v[48:49], v[44:45], v[40:41]\n"
      "v_pk_fma_f32 v[50:51], v[50:51], v[46:47], v[42:43]\n"
      "s_sub_u32 %[rem], %[rem], 1\n"
      "v_pk_fma_f32 v[44:45], v[44:45], v[52:53], v[44:45]\n"
      "v_pk_fma_f32 v[46:47], v[46:47], v[54:55], v[46:47]\n"
      "v_pk_fma_f32 v[40:41], v[40:41], v[52:53], v[48:49]\n"
      "v_pk_fma_f32 v[42:43], v[42:43], v[54:55], v[50:51]\n"
      "s_cbranch_scc1 Ltr0%=\n"
      "Lbk0%=:\n"
      "s_waitcnt vmcnt(6) lgkmcnt(5)\n"
      "v_mfma_f32_16x16x32_bf16 v[56:59], v[104:107], v[120:123], v[72:75]\n"
      "v_exp_f32 v52, v60\n"
      "v_exp_f32 v53, v61\n"
      "v_lshlrev_b64 v[48:49], 16, v[68:69]\n"
      "v_lshlrev_b64 v[50:51], 16, v[70:71]\n"
      "buffer_load_dwordx4 v[68:71], v[114:115], %[trs], 0 idxen offen sc1\n"
      "v_exp_f32 v54, v62\n"
      "v_exp_f32 v55, v63\n"
      "ds_read_b64 v[100:101], v36 offset:160\n"
      "ds_read_u16 v114, v37 offset:104\n"
      "v_pk_fma_f32 v[48:49], v[48:49], v[44:45], v[40:41]\n"
      "v_pk_fma_f32 v[50:51], v[50:51], v[46:47], v[42:43]\n"
      "s_sub_u32 %[rem], %[rem], 1\n"
      "v_pk_fma_f32 v[44:45], v[44:45], v[52:53], v[44:45]\n"
      "v_pk_fma_f32 v[46:47], v[46:47], v[54:55], v[46:47]\n"
      "v_pk_fma_f32 v[40:41], v[40:41], v[52:53], v[48:49]\n"
      "v_pk_fma_f32 v[42:43], v[42:43], v[54:55], v[50:51]\n"
      "s_cbranch_scc1 Ltr1%=\n"
      "Lbk1%=:\n"
      "s_waitcnt vmcnt(6) lgkmcnt(5)\n"
      "v_mfma_f32_16x16x32_bf16 v[60:63], v[108:111], v[120:123], v[76:79]\n"
      "v_exp_f32 v52, v56\n"
      "v_exp_f32 v53, v57\n"
      "v_lshlrev_b64 v[48:49], 16, v[72:73]\n"
      "v_lshlrev_b64 v[50:51], 16, v[74:75]\n"
      "buffer_load_dwordx4 v[72:75], v[116:117], %[trs], 0 idxen offen sc1\n"
      "v_exp_f32 v54, v58\n"
      "v_exp_f32 v55, v59\n"
      "ds_read_b64 v[104:105], v36 offset:192\n"
      "ds_read_u16 v116, v37 offset:112\n"
      "v_pk_fma_f32 v[48:49], v[48:49], v[44:45], v[40:41]\n"
      "v_pk_fma_f32 v[50:51], v[50:51], v[46:47], v[42:43]\n"
      "s_sub_u32 %[rem], %[rem], 1\n"
      "v_pk_fma_f32 v[44:45], v[44:45], v[52:53], v[44:45]\n"
      "v_pk_fma_f32 v[46:47], v[46:47], v[54:55], v[46:47]\n"
      "v_pk_fma_f32 v[40:41], v[40:41], v[52:53], v[48:49]\n"
      "v_pk_fma_f32 v[42:43], v[42:43], v[54:55], v[50:51]\n"
      "s_cbranch_scc1 Ltr2%=\n"
      "Lbk2%=:\n"
      "s_waitcnt vmcnt(6) lgkmcnt(5)\n"
      "v_mfma_f32_16x16x32_bf16 v[56:59], v[96:99], v[120:123], v[80:83]\n"
      "v_exp_f32 v52, v60\n"
      "v_exp_f32 v53, v61\n"
      "v_lshlrev_b64 v[48:49], 16, v[76:77]\n"
      "v_lshlrev_b64 v[50:51], 16, v[78:79]\n"
      "buffer_load_dwordx4 v[76:79], v[118:119], %[trs], 0 idxen offen sc1\n"
      "v_exp_f32 v54, v62\n"
      "v_exp_f32 v55, v63\n"
      "ds_read_b64 v[108:109], v36 offset:224\n"
      "ds_read_u16 v118, v37 offset:120\n"
      "v_pk_fma_f32 v[48:49], v[48:49], v[44:45], v[40:41]\n"
      "v_pk_fma_f32 v[50:51], v[50:51], v[46:47], v[42:43]\n"
      "s_sub_u32 %[rem], %[rem], 1\n"
      "v_pk_fma_f32 v[44:45], v[44:45], v[52:53], v[44:45]\n"
      "v_pk_fma_f32 v[46:47], v[46:47], v[54:55], v[46:47]\n"
      "v_pk_fma_f32 v[40:41], v[40:41], v[52:53], v[48:49]\n"
      "v_pk_fma_f32 v[42:43], v[42:43], v[54:55], v[50:51]\n"
      "s_cbranch_scc1 Ltr3%=\n"
      "Lbk3%=:\n"
      "s_sub_i32 %[tot], %[tot], 4\n"
      "s_cmp_gt_i32 %[tot], 26\n"
      "s_cbranch_scc1 Lp3%=\n"
      "s_setprio 2\n"
      "s_cmp_gt_i32 %[tot], 14\n"
      "s_cbranch_scc1 Lp3%=\n"
      "s_setprio 1\n"
      "s_cmp_gt_i32 %[tot], 6\n"
      "s_cbranch_scc1 Lp3%=\n"
      "s_setprio 0\n"
      "Lp3%=:\n"
      "s_sub_i32 %[prem], %[prem], 4\n"
      "s_cmp_gt_i32 %[prem], 8\n"
      "s_cbranch_scc0 Lsw4%=\n"
      "s_waitcnt vmcnt(6) lgkmcnt(5)\n"
      "v_mfma_f32_16x16x32_bf16 v[60:63], v[100:103], v[120:123], v[84:87]\n"
      "v_exp_f32 v52, v56\n"
      "v_exp_f32 v53, v57\n"
      "v_lshlrev_b64 v[48:49], 16, v[80:81]\n"
      "v_lshlrev_b64 v[50:51], 16, v[82:83]\n"
      "buffer_load_dwordx4 v[80:83], v[112:113], %[trs], 0 idxen offen sc1\n"
      "v_exp_f32 v54, v58\n"
      "v_exp_f32 v55, v59\n"
      "ds_read_b64 v[96:97], v36 offset:256\n"
      "ds_read_u16 v112, v37 offset:128\n"
      "v_pk_fma_f32 v[48:49], v[48:49], v[44:45], v[40:41]\n"
      "v_pk_fma_f32 v[50:51], v[50:51], v[46:47], v[42:43]\n"
      "s_sub_u32 %[rem], %[rem], 1\n"
      "v_pk_fma_f32 v[44:45], v[44:45], v[52:53], v[44:45]\n"
      "v_pk_fma_f32 v[46:47], v[46:47], v[54:55], v[46:47]\n"
      "v_pk_fma_f32 v[40:41], v[40:41], v[52:53], v[48:49]\n"
      "v_pk_fma_f32 v[42:43], v[42:43], v[54:55], v[50:51]\n"
      "s_cbranch_scc1 Ltr4%=\n"
      "Lbk4%=:\n"
      "s_waitcnt vmcnt(6) lgkmcnt(5)\n"
      "v_mfma_f32_16x16x32_bf16 v[56:59], v[104:107], v[120:123], v[88:91]\n"
      "v_exp_f32 v52, v60\n"
      "v_exp_f32 v53, v61\n"
      "v_lshlrev_b64 v[48:49], 16, v[84:85]\n"
      "v_lshlrev_b64 v[50:51], 16, v[86:87]\n"
      "buffer_load_dwordx4 v[84:87], v[114:115], %[trs], 0 idxen offen sc1\n"
      "v_exp_f32 v54, v62\n"
      "v_exp_f32 v55, v63\n"
      "ds_read_b64 v[100:101], v36 offset:288\n"
      "ds_read_u16 v114, v37 offset:136\n"
      "v_pk_fma_f32 v[48:49], v[48:49], v[44:45], v[40:41]\n"
      "v_pk_fma_f32 v[50:51], v[50:51], v[46:47], v[42:43]\n"
      "s_sub_u32 %[rem], %[rem], 1\n"
      "v_pk_fma_f32 v[44:45], v[44:45], v[52:53], v[44:45]\n"
      "v_pk_fma_f32 v[46:47], v[46:47], v[54:55], v[46:47]\n"
      "v_pk_fma_f32 v[40:41], v[40:41], v[52:53], v[48:49]\n"
      "v_pk_fma_f32 v[42:43], v[42:43], v[54:55], v[50:51]\n"
      "s_cbranch_scc1 Ltr5%=\n"
      "Lbk5%=:\n"
      "s_waitcnt vmcnt(6) lgkmcnt(5)\n"
      "v_mfma_f32_16x16x32_bf16 v[60:63], v[108:111], v[120:123], v[92:95]\n"
      "v_exp_f32 v52, v56\n"
      "v_exp_f32 v53, v57\n"
      "v_lshlrev_b64 v[48:49], 16, v[88:89]\n"
      "v_lshlrev_b64 v[50:51], 16, v[90:91]\n"
      "buffer_load_dwordx4 v[88:91], v[116:117], %[trs], 0 idxen offen sc1\n"
      "v_exp_f32 v54, v58\n"
      "v_exp_f32 v55, v59\n"
      "ds_read_b64 v[104:105], v36 offset:320\n"
      "ds_read_u16 v116, v37 offset:144\n"
      "v_pk_fma_f32 v[48:49], v[48:49], v[44:45], v[40:41]\n"
      "v_pk_fma_f32 v[50:51], v[50:51], v[46:47], v[42:43]\n"
      "s_sub_u32 %[rem], %[rem], 1\n"
      "v_pk_fma_f32 v[44:45], v[44:45], v[52:53], v[44:45]\n"
      "v_pk_fma_f32 v[46:47], v[46:47], v[54:55], v[46:47]\n"
      "v_pk_fma_f32 v[40:41], v[40:41], v[52:53], v[48:49]\n"
      "v_pk_fma_f32 v[42:43], v[42:43], v[54:55], v[50:51]\n"
      "s_cbranch_scc1 Ltr6%=\n"
      "Lbk6%=:\n"
      "s_waitcnt vmcnt(6) lgkmcnt(5)\n"
      "v_mfma_f32_16x16x32_bf16 v[56:59], v[96:99], v[120:123], v[64:67]\n"
      "v_exp_f32 v52, v60\n"
      "v_exp_f32 v53, v61\n"
      "v_lshlrev_b64 v[48:49], 16, v[92:93]\n"
      "v_lshlrev_b64 v[50:51], 16, v[94:95]\n"
      "buffer_load_dwordx4 v[92:95], v[118:119], %[trs], 0 idxen offen sc1\n"
      "v_exp_f32 v54, v62\n"
      "v_exp_f32 v55, v63\n"
      "ds_read_b64 v[108:109], v36 offset:352\n"
      "ds_read_u16 v118, v37 offset:152\n"
      "v_pk_fma_f32 v[48:49], v[48:49], v[44:45], v[40:41]\n"
      "v_pk_fma_f32 v[50:51], v[50:51], v[46:47], v[42:43]\n"
      "s_sub_u32 %[rem], %[rem], 1\n"
      "v_pk_fma_f32 v[44:45], v[44:45], v[52:53], v[44:45]\n"
      "v_pk_fma_f32 v[46:47], v[46:47], v[54:55], v[46:47]\n"
      "v_pk_fma_f32 v[40:41], v[40:41], v[52:53], v[48:49]\n"
      "v_pk_fma_f32 v[42:43], v[42:43], v[54:55], v[50:51]\n"
      "s_cbranch_scc1 Ltr7%=\n"
      "Lbk7%=:\n"
      "s_sub_i32 %[tot], %[tot], 4\n"
      "s_cmp_gt_i32 %[tot], 26\n"
      "s_cbranch_scc1 Lp7%=\n"
      "s_setprio 2\n"
      "s_cmp_gt_i32 %[tot], 14\n"
      "s_cbranch_scc1 Lp7%=\n"
      "s_setprio 1\n"
      "s_cmp_gt_i32 %[tot], 6\n"
      "s_cbranch_scc1 Lp7%=\n"
      "s_setprio 0\n"
      "Lp7%=:\n"
      "v_add_u32 v36, v36, v38\n"
      "v_add_u32 v37, 64, v37\n"
      "s_sub_i32 %[prem], %[prem], 4\n"
      "s_cmp_gt_i32 %[prem], 8\n"
      "s_cbranch_scc1 Lloop%=\n"
      "s_waitcnt vmcnt(3)\n"
      "Ldr0%=:\n"
      "s_waitcnt lgkmcnt(2)\n"
      "v_mfma_f32_16x16x32_bf16 v[60:63], v[100:103], v[120:123], v[68:71]\n"
      "v_exp_f32 v52, v56\n"
      "v_exp_f32 v53, v57\n"
      "v_lshlrev_b64 v[48:49], 16, v[64:65]\n"
      "v_lshlrev_b64 v[50:51], 16, v[66:67]\n"
      "v_exp_f32 v54, v58\n"
      "v_exp_f32 v55, v59\n"
      "ds_read_b64 v[96:97], v36 offset:128\n"
      "v_pk_fma_f32 v[48:49], v[48:49], v[44:45], v[40:41]\n"
      "v_pk_fma_f32 v[50:51], v[50:51], v[46:47], v[42:43]\n"
      "s_sub_u32 %[rem], %[rem], 1\n"
      "v_pk_fma_f32 v[44:45], v[44:45], v[52:53], v[44:45]\n"
      "v_pk_fma_f32 v[46:47], v[46:47], v[54:55], v[46:47]\n"
      "v_pk_fma_f32 v[40:41], v[40:41], v[52:53], v[48:49]\n"
      "v_pk_fma_f32 v[42:43], v[42:43], v[54:55], v[50:51]\n"
      "s_cbranch_scc1 Ltrd0%=\n"
      "Lbkd0%=:\n"
      "s_waitcnt lgkmcnt(2)\n"
      "v_mfma_f32_16x16x32_bf16 v[56:59], v[104:107], v[120:123], v[72:75]\n"
      "v_exp_f32 v52, v60\n"
      "v_exp_f32 v53, v61\n"
      "v_lshlrev_b64 v[48:49], 16, v[68:69]\n"
      "v_lshlrev_b64 v[50:51], 16, v[70:71]\n"
      "v_exp_f32 v54, v62\n"
      "v_exp_f32 v55, v63\n"
      "ds_read_b64 v[100:101], v36 offset:160\n"
      "v_pk_fma_f32 v[48:49], v[48:49], v[44:45], v[40:41]\n"
      "v_pk_fma_f32 v[50:51], v[50:51], v[46:47], v[42:43]\n"
      "s_sub_u32 %[rem], %[rem], 1\n"
      "v_pk_fma_f32 v[44:45], v[44:45], v[52:53], v[44:45]\n"
      "v_pk_fma_f32 v[46:47], v[46:47], v[54:55], v[46:47]\n"
      "v_pk_fma_f32 v[40:41], v[40:41], v[52:53], v[48:49]\n"
      "v_pk_fma_f32 v[42:43], v[42:43], v[54:55], v[50:51]\n"
      "s_cbranch_scc1 Ltrd1%=\n"
      "Lbkd1%=:\n"
      "s_waitcnt lgkmcnt(2)\n"
      "v_mfma_f32_16x16x32_bf16 v[60:63], v[108:111], v[120:123], v[76:79]\n"
      "v_exp_f32 v52, v56\n"
      "v_exp_f32 v53, v57\n"
      "v_lshlrev_b64 v[48:49], 16, v[72:73]\n"
      "v_lshlrev_b64 v[50:51], 16, v[74:75]\n"
      "v_exp_f32 v54, v58\n"
      "v_exp_f32 v55, v59\n"
      "ds_read_b64 v[104:105], v36 offset:192\n"
      "v_pk_fma_f32 v[48:49], v[48:49], v[44:45], v[40:41]\n"
      "v_pk_fma_f32 v[50:51], v[50:51], v[46:47], v[42:43]\n"
      "s_sub_u32 %[rem], %[rem], 1\n"
      "v_pk_fma_f32 v[44:45], v[44:45], v[52:53], v[44:45]\n"
      "v_pk_fma_f32 v[46:47], v[46:47], v[54:55], v[46:47]\n"
      "v_pk_fma_f32 v[40:41], v[40:41], v[52:53], v[48:49]\n"
      "v_pk_fma_f32 v[42:43], v[42:43], v[54:55], v[50:51]\n"
      "s_cbranch_scc1 Ltrd2%=\n"
      "Lbkd2%=:\n"
      "s_waitcnt lgkmcnt(2)\n"
      "v_mfma_f32_16x16x32_bf16 v[56:59], v[96:99], v[120:123], v[80:83]\n"
      "v_exp_f32 v52, v60\n"
      "v_exp_f32 v53, v61\n"
      "v_lshlrev_b64 v[48:49], 16, v[76:77]\n"
      "v_lshlrev_b64 v[50:51], 16, v[78:79]\n"
      "v_exp_f32 v54, v62\n"
      "v_exp_f32 v55, v63\n"
      "ds_read_b64 v[108:109], v36 offset:224\n"
      "v_pk_fma_f32 v[48:49], v[48:49], v[44:45], v[40:41]\n"
      "v_pk_fma_f32 v[50:51], v[50:51], v[46:47], v[42:43]\n"
      "s_sub_u32 %[rem], %[rem], 1\n"
      "v_pk_fma_f32 v[44:45], v[44:45], v[52:53], v[44:45]\n"
      "v_pk_fma_f32 v[46:47], v[46:47], v[54:55], v[46:47]\n"
      "v_pk_fma_f32 v[40:41], v[40:41], v[52:53], v[48:49]\n"
      "v_pk_fma_f32 v[42:43], v[42:43], v[54:55], v[50:51]\n"
      "s_cbranch_scc1 Ltrd3%=\n"
      "Lbkd3%=:\n"
      "s_sub_i32 %[tot], %[tot], 4\n"
      "s_cmp_gt_i32 %[tot], 26\n"
      "s_cbranch_scc1 Lpd3%=\n"
      "s_setprio 2\n"
      "s_cmp_gt_i32 %[tot], 14\n"
      "s_cbranch_scc1 Lpd3%=\n"
      "s_setprio 1\n"
      "s_cmp_gt_i32 %[tot], 6\n"
      "s_cbranch_scc1 Lpd3%=\n"
      "s_setprio 0\n"
      "Lpd3%=:\n"
      "s_waitcnt vmcnt(0)\n"
      "Ldr4%=:\n"
      "s_waitcnt lgkmcnt(2)\n"
      "v_mfma_f32_16x16x32_bf16 v[60:63], v[100:103], v[120:123], v[84:87]\n"
      "v_exp_f32 v52, v56\n"
      "v_exp_f32 v53, v57\n"
      "v_lshlrev_b64 v[48:49], 16, v[80:81]\n"
      "v_lshlrev_b64 v[50:51], 16, v[82:83]\n"
      "v_exp_f32 v54, v58\n"
      "v_exp_f32 v55, v59\n"
      "ds_read_b64 v[96:97], v36 offset:256\n"
      "v_pk_fma_f32 v[48:49], v[48:49], v[44:45], v[40:41]\n"
      "v_pk_fma_f32 v[50:51], v[50:51], v[46:47], v[42:43]\n"
      "s_sub_u32 %[rem], %[rem], 1\n"
      "v_pk_fma_f32 v[44:45], v[44:45], v[52:53], v[44:45]\n"
      "v_pk_fma_f32 v[46:47], v[46:47], v[54:55], v[46:47]\n"
      "v_pk_fma_f32 v[40:41], v[40:41], v[52:53], v[48:49]\n"
      "v_pk_fma_f32 v[42:43], v[42:43], v[54:55], v[50:51]\n"
      "s_cbranch_scc1 Ltrd4%=\n"
      "Lbkd4%=:\n"
      "s_waitcnt lgkmcnt(2)\n"
      "v_mfma_f32_16x16x32_bf16 v[56:59], v[104:107], v[120:123], v[88:91]\n"
      "v_exp_f32 v52, v60\n"
      "v_exp_f32 v53, v61\n"
      "v_lshlrev_b64 v[48:49], 16, v[84:85]\n"
      "v_lshlrev_b64 v[50:51], 16, v[86:87]\n"
      "v_exp_f32 v54, v62\n"
      "v_exp_f32 v55, v63\n"
      "ds_read_b64 v[100:101], v36 offset:288\n"
      "v_pk_fma_f32 v[48:49], v[48:49], v[44:45], v[40:41]\n"
      "v_pk_fma_f32 v[50:51], v[50:51], v[46:47], v[42:43]\n"
      "s_sub_u32 %[rem], %[rem], 1\n"
      "v_pk_fma_f32 v[44:45], v[44:45], v[52:53], v[44:45]\n"
      "v_pk_fma_f32 v[46:47], v[46:47], v[54:55], v[46:47]\n"
      "v_pk_fma_f32 v[40:41], v[40:41], v[52:53], v[48:49]\n"
      "v_pk_fma_f32 v[42:43], v[42:43], v[54:55], v[50:51]\n"
      "s_cbranch_scc1 Ltrd5%=\n"
      "Lbkd5%=:\n"
      "s_waitcnt lgkmcnt(2)\n"
      "v_mfma_f32_16x16x32_bf16 v[60:63], v[108:111], v[120:123], v[92:95]\n"
      "v_exp_f32 v52, v56\n"
      "v_exp_f32 v53, v57\n"
      "v_lshlrev_b64 v[48:49], 16, v[88:89]\n"
      "v_lshlrev_b64 v[50:51], 16, v[90:91]\n"
      "v_exp_f32 v54, v58\n"
      "v_exp_f32 v55, v59\n"
      "ds_read_b64 v[104:105], v36 offset:320\n"
      "v_pk_fma_f32 v[48:49], v[48:49], v[44:45], v[40:41]\n"
      "v_pk_fma_f32 v[50:51], v[50:51], v[46:47], v[42:43]\n"
      "s_sub_u32 %[rem], %[rem], 1\n"
      "v_pk_fma_f32 v[44:45], v[44:45], v[52:53], v[44:45]\n"
      "v_pk_fma_f32 v[46:47], v[46:47], v[54:55], v[46:47]\n"
      "v_pk_fma_f32 v[40:41], v[40:41], v[52:53], v[48:49]\n"
      "v_pk_fma_f32 v[42:43], v[42:43], v[54:55], v[50:51]\n"
      "s_cbranch_scc1 Ltrd6%=\n"
      "Lbkd6%=:\n"
      "s_waitcnt lgkmcnt(2)\n"
      "v_mfma_f32_16x16x32_bf16 v[56:59], v[96:99], v[120:123], v[64:67]\n"
      "v_exp_f32 v52, v60\n"
      "v_exp_f32 v53, v61\n"
      "v_lshlrev_b64 v[48:49], 16, v[92:93]\n"
      "v_lshlrev_b64 v[50:51], 16, v[94:95]\n"
      "v_exp_f32 v54, v62\n"
      "v_exp_f32 v55, v63\n"
      "ds_read_b64 v[108:109], v36 offset:352\n"
      "v_pk_fma_f32 v[48:49], v[48:49], v[44:45], v[40:41]\n"
      "v_pk_fma_f32 v[50:51], v[50:51], v[46:47], v[42:43]\n"
      "s_sub_u32 %[rem], %[rem], 1\n"
      "v_pk_fma_f32 v[44:45], v[44:45], v[52:53], v[44:45]\n"
      "v_pk_fma_f32 v[46:47], v[46:47], v[54:55], v[46:47]\n"
      "v_pk_fma_f32 v[40:41], v[40:41], v[52:53], v[48:49]\n"
      "v_pk_fma_f32 v[42:43], v[42:43], v[54:55], v[50:51]\n"
      "s_cbranch_scc1 Ltrd7%=\n"
      "Lbkd7%=:\n"
      "s_sub_i32 %[tot], %[tot], 4\n"
      "s_cmp_gt_i32 %[tot], 26\n"
      "s_cbranch_scc1 Lpd7%=\n"
      "s_setprio 2\n"
      "s_cmp_gt_i32 %[tot], 14\n"
      "s_cbranch_scc1 Lpd7%=\n"
      "s_setprio 1\n"
      "s_cmp_gt_i32 %[tot], 6\n"
      "s_cbranch_scc1 Lpd7%=\n"
      "s_setprio 0\n"
      "Lpd7%=:\n"
      "v_add_u32 v36, v36, v38\n"
      "s_waitcnt vmcnt(0)\n"
      "s_branch Ldr0%=\n"
      "Lsw4%=:\n"
      "s_waitcnt vmcnt(3)\n"
      "s_branch Ldr4%=\n"
      "Ltr0%=:\n"
      "s_call_b64 %[ret], Lepi%=\n"
      "v_mfma_f32_16x16x32_bf16 v[60:63], v[100:103], v[120:123], v[68:71]\n"
      "s_nop 5\n"
      "s_branch Lbk0%=\n"
      "Ltr1%=:\n"
      "s_call_b64 %[ret], Lepi%=\n"
      "v_mfma_f32_16x16x32_bf16 v[56:59], v[104:107], v[120:123], v[72:75]\n"
      "s_nop 5\n"
      "s_branch Lbk1%=\n"
      "Ltr2%=:\n"
      "s_call_b64 %[ret], Lepi%=\n"
      "v_mfma_f32_16x16x32_bf16 v[60:63], v[108:111], v[120:123], v[76:79]\n"
      "s_nop 5\n"
      "s_branch Lbk2%=\n"
      "Ltr3%=:\n"
      "s_call_b64 %[ret], Lepi%=\n"
      "v_mfma_f32_16x16x32_bf16 v[56:59], v[96:99], v[120:123], v[80:83]\n"
      "s_nop 5\n"
      "s_branch Lbk3%=\n"
      "Ltr4%=:\n"
      "s_call_b64 %[ret], Lepi%=\n"
      "v_mfma_f32_16x16x32_bf16 v[60:63], v[100:103], v[120:123], v[84:87]\n"
      "s_nop 5\n"
      "s_branch Lbk4%=\n"
      "Ltr5%=:\n"
      "s_call_b64 %[ret], Lepi%=\n"
      "v_mfma_f32_16x16x32_bf16 v[56:59], v[104:107], v[120:123], v[88:91]\n"
      "s_nop 5\n"
      "s_branch Lbk5%=\n"
      "Ltr6%=:\n"
      "s_call_b64 %[ret], Lepi%=\n"
      "v_mfma_f32_16x16x32_bf16 v[60:63], v[108:111], v[120:123], v[92:95]\n"
      "s_nop 5\n"
      "s_branch Lbk6%=\n"
      "Ltr7%=:\n"
      "s_call_b64 %[ret], Lepi%=\n"
      "v_mfma_f32_16x16x32_bf16 v[56:59], v[96:99], v[120:123], v[64:67]\n"
      "s_nop 5\n"
      "s_branch Lbk7%=\n"
      "Ltrd0%=:\n"
      "s_call_b64 %[ret], Lepi%=\n"
      "v_mfma_f32_16x16x32_bf16 v[60:63], v[100:103], v[120:123], v[68:71]\n"
      "s_nop 5\n"
      "s_branch Lbkd0%=\n"
      "Ltrd1%=:\n"
      "s_call_b64 %[ret], Lepi%=\n"
      "v_mfma_f32_16x16x32_bf16 v[56:59], v[104:107], v[120:123], v[72:75]\n"
      "s_nop 5\n"
      "s_branch Lbkd1%=\n"
      "Ltrd2%=:\n"
      "s_call_b64 %[ret], Lepi%=\n"
      "v_mfma_f32_16x16x32_bf16 v[60:63], v[108:111], v[120:123], v[76:79]\n"
      "s_nop 5\n"
      "s_branch Lbkd2%=\n"
      "Ltrd3%=:\n"
      "s_call_b64 %[ret], Lepi%=\n"
      "v_mfma_f32_16x16x32_bf16 v[56:59], v[96:99], v[120:123], v[80:83]\n"
      "s_nop 5\n"
      "s_branch Lbkd3%=\n"
      "Ltrd4%=:\n"
      "s_call_b64 %[ret], Lepi%=\n"
      "v_mfma_f32_16x16x32_bf16 v[60:63], v[100:103], v[120:123], v[84:87]\n"
      "s_nop 5\n"
      "s_branch Lbkd4%=\n"
      "Ltrd5%=:\n"
      "s_call_b64 %[ret], Lepi%=\n"
      "v_mfma_f32_16x16x32_bf16 v[56:59], v[104:107], v[120:123], v[88:91]\n"
      "s_nop 5\n"
      "s_branch Lbkd5%=\n"
      "Ltrd6%=:\n"
      "s_call_b64 %[ret], Lepi%=\n"
      "v_mfma_f32_16x16x32_bf16 v[60:63], v[108:111], v[120:123], v[92:95]\n"
      "s_nop 5\n"
      "s_branch Lbkd6%=\n"
      "Ltrd7%=:\n"
      "s_call_b64 %[ret], Lepi%=\n"
      "v_mfma_f32_16x16x32_bf16 v[56:59], v[96:99], v[120:123], v[64:67]\n"
      "s_nop 5\n"
      "s_branch Lbkd7%=\n"
      "Lepi%=:\n"
      "v_rcp_f32 v52, v44\n"
      "v_rcp_f32 v53, v45\n"
      "v_rcp_f32 v54, v46\n"
      "v_rcp_f32 v55, v47\n"
      "v_pk_add_f32 v[124:125], v[124:125], v[44:45]\n"
      "v_pk_add_f32 v[124:125], v[124:125], v[46:47]\n"
      "v_pk_fma_f32 v[124:125], v[40:41], 0, v[124:125] op_sel_hi:[1,0,1]\n"
      "v_pk_fma_f32 v[124:125], v[42:43], 0, v[124:125] op_sel_hi:[1,0,1]\n"
      "s_cmp_eq_u32 %[k], 1\n"
      "s_cbranch_scc1 Le1%=\n"
      "s_cmp_eq_u32 %[k], 2\n"
      "s_cbranch_scc1 Le2%=\n"
      "Le0%=:\n"
      "v_pk_fma_f32 v[24:25], v[40:41], v[52:53], v[24:25]\n"
      "v_pk_fma_f32 v[26:27], v[42:43], v[54:55], v[26:27]\n"
      "v_mov_b64 v[40:41], 0\n"
      "v_mov_b64 v[42:43], 0\n"
      "v_mov_b64 v[44:45], %[ones]\n"
      "v_mov_b64 v[46:47], %[ones]\n"
      "v_pk_mul_f32 v[52:53], v[24:25], %[nl2]\n"
      "v_pk_mul_f32 v[54:55], v[26:27], %[nl2]\n"
      "v_exp_f32 v52, v52\n"
      "v_exp_f32 v53, v53\n"
      "v_exp_f32 v54, v54\n"
      "v_exp_f32 v55, v55\n"
      "s_nop 0\n"
      "v_pk_add_f32 v[52:53], v[52:53], %[ones]\n"
      "v_pk_add_f32 v[54:55], v[54:55], %[ones]\n"
      "v_rcp_f32 v52, v52\n"
      "v_rcp_f32 v53, v53\n"
      "v_rcp_f32 v54, v54\n"
      "v_rcp_f32 v55, v55\n"
      "s_nop 0\n"
      "v_pk_mul_f32 v[48:49], v[24:25], v[52:53]\n"
      "v_pk_mul_f32 v[50:51], v[26:27], v[54:55]\n"
      "v_cvt_pk_bf16_f32 v52, v48, v49\n"
      "v_cvt_pk_bf16_f32 v53, v50, v51\n"
      "buffer_store_dwordx2 v[52:53], v15, %[ors], 0 offen sc1\n"
      "s_branch Lnx1%=\n"
      "Le1%=:\n"
      "v_pk_fma_f32 v[28:29], v[40:41], v[52:53], v[28:29]\n"
      "v_pk_fma_f32 v[30:31], v[42:43], v[54:55], v[30:31]\n"
      "v_mov_b64 v[40:41], 0\n"
      "v_mov_b64 v[42:43], 0\n"
      "v_mov_b64 v[44:45], %[ones]\n"
      "v_mov_b64 v[46:47], %[ones]\n"
      "v_pk_mul_f32 v[52:53], v[28:29], %[nl2]\n"
      "v_pk_mul_f32 v[54:55], v[30:31], %[nl2]\n"
      "v_exp_f32 v52, v52\n"
      "v_exp_f32 v53, v53\n"
      "v_exp_f32 v54, v54\n"
      "v_exp_f32 v55, v55\n"
      "s_nop 0\n"
      "v_pk_add_f32 v[52:53], v[52:53], %[ones]\n"
      "v_pk_add_f32 v[54:55], v[54:55], %[ones]\n"
      "v_rcp_f32 v52, v52\n"
      "v_rcp_f32 v53, v53\n"
      "v_rcp_f32 v54, v54\n"
      "v_rcp_f32 v55, v55\n"
      "s_nop 0\n"
      "v_pk_mul_f32 v[48:49], v[28:29], v[52:53]\n"
      "v_pk_mul_f32 v[50:51], v[30:31], v[54:55]\n"
      "v_cvt_pk_bf16_f32 v52, v48, v49\n"
      "v_cvt_pk_bf16_f32 v53, v50, v51\n"
      "buffer_store_dwordx2 v[52:53], v16, %[ors], 0 offen sc1\n"
      "s_branch Lnx2%=\n"
      "Le2%=:\n"
      "v_pk_fma_f32 v[32:33], v[40:41], v[52:53], v[32:33]\n"
      "v_pk_fma_f32 v[34:35], v[42:43], v[54:55], v[34:35]\n"
      "v_mov_b64 v[40:41], 0\n"
      "v_mov_b64 v[42:43], 0\n"
      "v_mov_b64 v[44:45], %[ones]\n"
      "v_mov_b64 v[46:47], %[ones]\n"
      "v_pk_mul_f32 v[52:53], v[32:33], %[nl2]\n"
      "v_pk_mul_f32 v[54:55], v[34:35], %[nl2]\n"
      "v_exp_f32 v52, v52\n"
      "v_exp_f32 v53, v53\n"
      "v_exp_f32 v54, v54\n"
      "v_exp_f32 v55, v55\n"
      "s_nop 0\n"
      "v_pk_add_f32 v[52:53], v[52:53], %[ones]\n"
      "v_pk_add_f32 v[54:55], v[54:55], %[ones]\n"
      "v_rcp_f32 v52, v52\n"
      "v_rcp_f32 v53, v53\n"
      "v_rcp_f32 v54, v54\n"
      "v_rcp_f32 v55, v55\n"
      "s_nop 0\n"
      "v_pk_mul_f32 v[48:49], v[32:33], v[52:53]\n"
      "v_pk_mul_f32 v[50:51], v[34:35], v[54:55]\n"
      "v_cvt_pk_bf16_f32 v52, v48, v49\n"
      "v_cvt_pk_bf16_f32 v53, v50, v51\n"
      "buffer_store_dwordx2 v[52:53], v17, %[ors], 0 offen sc1\n"
      "s_branch Lnx3%=\n"
      "Lnx0%=:\n"
      "s_bfe_u32 %[rem], %[lens], 0xa0000\n"
      "s_cbranch_scc0 Lnx1%=\n"
      "v_mov_b64 v[122:123], v[18:19]\n"
      "s_sub_u32 %[rem], %[rem], 1\n"
      "s_mov_b32 %[k], 0\n"
      "s_setpc_b64 %[ret]\n"
      "Lnx1%=:\n"
      "s_bfe_u32 %[rem], %[lens], 0xa000a\n"
      "s_cbranch_scc0 Lnx2%=\n"
      "v_mov_b64 v[122:123], v[20:21]\n"
      "s_sub_u32 %[rem], %[rem], 1\n"
      "s_mov_b32 %[k], 1\n"
      "s_setpc_b64 %[ret]\n"
      "Lnx2%=:\n"
      "s_bfe_u32 %[rem], %[lens], 0xa0014\n"
      "s_cbranch_scc0 Lnx3%=\n"
      "v_mov_b64 v[122:123], v[22:23]\n"
      "s_sub_u32 %[rem], %[rem], 1\n"
      "s_mov_b32 %[k], 2\n"
      "s_setpc_b64 %[ret]\n"
      "Lnx3%=:\n"
      "s_waitcnt vmcnt(0) lgkmcnt(0)\n"
          : [tot] "+&s"(tot), [prem] "+&s"(prem), [rem] "=&s"(rem), [k] "=&s"(kk), [ret] "=&s"(ret), "+{v36}"(v_ea), "+{v37}"(v_idx),
            "+{v[24:27]}"(yq[0]), "+{v[28:31]}"(yq[1]), "+{v[32:35]}"(yq[2]), "+{v[124:125]}"(chk)
          : [trs] "s"(trs), [ors] "s"(ors), [lens] "s"(l0 | l1 << 10 | l2 << 20), [ones] "s"(0x3f8000003f800000ull),
            [nl2] "s"(0xbfb8aa3bbfb8aa3bull), "{v38}"(v_eainc), "{v39}"(j * 16), "{v[120:121]}"(cb), "{v[18:19]}"(bq[0]),
            "{v[20:21]}"(bq[1]), "{v[22:23]}"(bq[2]), "{v15}"(oq[0]), "{v16}"(oq[1]), "{v17}"(oq[2]), "{v[126:127]}"(oh)
          : "memory", "scc", "v40", "v41", "v42", "v43", "v44", "v45", "v46", "v47", "v48", "v49", "v50", "v51", "v52", "v53", "v54", "v55", "v56", "v57", "v58", "v59", "v60", "v61", "v62", "v63", "v64", "v65", "v66", "v67", "v68", "v69", "v70", "v71", "v72", "v73", "v74", "v75", "v76", "v77", "v78", "v79", "v80", "v81", "v82", "v83", "v84", "v85", "v86", "v87", "v88", "v89", "v90", "v91", "v92", "v93", "v94", "v95", "v96", "v97", "v98", "v99", "v100", "v101", "v102", "v103", "v104", "v105", "v106", "v107", "v108", "v109", "v110", "v111", "v112", "v113", "v114", "v115", "v116", "v117", "v118", "v119", "v122", "v123");
      if (__any(!(chk.x + chk.y < 1e36f))) {
        const int ln = fresh_lane();
        const int q = ln >> 4, j = ln & 15;
        const u16* cw = (const u16*)p.gw + 512 * 256 + s * 64 + j * 4;
        const auto trs0 = RSRC(p.T + (size_t)s * p.N * 64, 0, p.N * 256);
        int t0 = wb;
#pragma unroll 1
        for (int k = 0; k < 3; k++) {
          const int len = k == 0 ? l0 : (k == 1 ? l1 : l2), qd = k == 0 ? wq : (k == 1 ? 31 - wq : 47 - wq);
          if (len == 0) break;
          const u32 mine = *(LAS u16*)(S + L_ORDER + qd * 8 + q * 2);
          const u32x2 xa = *(LAS u32x2*)(S + L_XA + min(mine, 159u) * AUP + j * 8);
          const u32x2 xu = *(LAS u32x2*)(S + L_XU + min(mine, 159u) * AUP + j * 8);
          float acc[4] = {u2f(xu.x << 16), u2f(xu.x & 0xffff0000u), u2f(xu.y << 16), u2f(xu.y & 0xffff0000u)};
          const float za[4] = {u2f(xa.x << 16), u2f(xa.x & 0xffff0000u), u2f(xa.y << 16), u2f(xa.y & 0xffff0000u)};
#pragma unroll 1
          for (int t = t0; t < t0 + len; t++) {
            const u32 id = *(LAS u16*)(S + L_IDX + (t * 4 + wq * 32 + q) * 2);
            const u32x2 e = *(LAS u32x2*)(S + L_EA + 512 + (t * 4 + q) * 8);
            const u32x4 g = __builtin_amdgcn_raw_buffer_load_b128(trs0, id * 256 + j * 16, 0, 16);
            const float hv[4] = {u2f(g.x << 16), u2f(g.y << 16 | g.x >> 16), u2f(g.z << 16), u2f(g.w << 16 | g.z >> 16)};
#pragma unroll
            for (int r = 0; r < 4; r++) {
              float ec = u2f(e.x << 16) * u2f((u32)cw[r] << 16) + u2f(e.x & 0xffff0000u) * u2f((u32)cw[256 + r] << 16) +
                         u2f(e.y << 16) * u2f((u32)cw[512 + r] << 16) + u2f(e.y & 0xffff0000u) * u2f((u32)cw[768 + r] << 16);
              float z = u2f(g[r]) + za[r] + ec * NLOG2E;
              acc[r] += hv[r] * __builtin_amdgcn_rcpf(1.0f + __builtin_amdgcn_exp2f(z));
            }
          }
          t0 += len;
          __builtin_amdgcn_raw_buffer_store_b64(u32x2{pk_bf16(silu_f(acc[0]), silu_f(acc[1])), pk_bf16(silu_f(acc[2]), silu_f(acc[3]))},
                                                ors, mine < 160u ? (n0 + mine) * 512u + s * 128 + j * 8 : OOB, 0, 0);
        }
      }
    }
  } else {
    LAS float* agg = (LAS float*)(S + L_X);
    for (int i = tid; i < 160 * 64; i += 1024) agg[i] = 0.f;
    WAITGE(C_TRDY, 1);
    __syncthreads();
    const u16* cw = (const u16*)p.gw + 512 * 256 + s * 64 + lane;
    const float c0 = u2f((u32)cw[0] << 16), c1 = u2f((u32)cw[256] << 16), c2 = u2f((u32)cw[512] << 16),
                c3 = u2f((u32)cw[768] << 16);
    const auto trs = RSRC(p.T + (size_t)s * p.N * 64, 0, p.N * 256);
#pragma unroll 1
    for (int eb = w * 64; eb < p.E; eb += 1024) {
      const int e = eb + lane;
      const int dl = e < p.E ? p.dst[e] - n0 : -1;
      u64 m = __ballot(dl >= 0 && dl < nrows);
      while (m) {
        const int k = __builtin_ctzll(m);
        m &= m - 1;
        const int dk = __shfl(dl, k);
        const u32 so = (u32)p.src[eb + k] * 256u;
        const u32 ax = p.ea[(eb + k) * 2], ay = p.ea[(eb + k) * 2 + 1];
        const u32 wd = __builtin_amdgcn_raw_buffer_load_b32(trs, so + lane * 4, 0, 16);
        const u32 wp = __builtin_amdgcn_raw_buffer_load_b32(trs, so + (lane & ~1) * 4, 0, 16);
        const float hv = (lane & 1) ? u2f(wd << 16 | wp >> 16) : u2f(wd << 16);
        const float ec = u2f(ax << 16) * c0 + u2f(ax & 0xffff0000u) * c1 + u2f(ay << 16) * c2 + u2f(ay & 0xffff0000u) * c3;
        const float z = u2f(wd) + u2f((u32) * (LAS u16*)(S + L_XA + dk * AUP + lane * 2) << 16) + ec * NLOG2E;
        __hip_atomic_fetch_add(agg + dk * 64 + lane, hv * __builtin_amdgcn_rcpf(1.0f + __builtin_amdgcn_exp2f(z)),
                               __ATOMIC_RELAXED, __HIP_MEMORY_SCOPE_WORKGROUP);
      }
    }
    __syncthreads();
    for (int i = tid; i < nrows * 64; i += 1024) {
      const float y = agg[i] + u2f((u32) * (LAS u16*)(S + L_XU + (i >> 6) * AUP + (i & 63) * 2) << 16);
      p.out[(size_t)(n0 + (i >> 6)) * D + s * 64 + (i & 63)] = (u16)pk_bf16(silu_f(y), 0.f);
    }
  }
}

extern "C" void kernel_launch(void* const* d_in, const int* in_sizes, int n_in, void* d_out, int out_size,
                              void* d_ws, size_t ws_size, hipStream_t stream) {
  Params p;
  p.x = (const u32*)d_in[0];
  p.ea = (const u32*)d_in[1];
  p.src = (const int*)d_in[2];
  p.dst = (const int*)d_in[3];
  p.uw = (const u32*)d_in[4];
  p.vw = (const u32*)d_in[5];
  p.gw = (const u32*)d_in[6];
  p.N = in_sizes[0] / D;
  p.E = in_sizes[2];
  p.BS = (p.N + NB - 1) / NB;
  p.EPP = (p.E + NWG - 1) / NWG;
  p.magic = (u32)(0x100000000ull / (u64)p.BS) + 1u;
  p.pad = 0;
  char* ws = (char*)d_ws;
  size_t o = 0;
  p.gen = (u64*)(ws + o); o += 256;
  p.tflag = (u64*)(ws + o); o += 4 * NB * 64;
  p.gran = (u64*)(ws + o); o += (size_t)NB * NWG * 8;
  p.recs = (u32*)(ws + o); o += (size_t)NWG * RECSTRIDE;
  p.T = (u32*)(ws + o);
  p.out = (u16*)d_out;
  fused<<<NWG, 1024, 0, stream>>>(p);
}
```
